```python
import torch

def _softmax(x, axis=-1):
    return torch.softmax(x, dim=axis)
def _gelu(x):
    return torch.nn.functional.gelu(x, approximate="tanh")
def _silu(x):
    return torch.nn.functional.silu(x)
def _sigmoid(x):
    return torch.sigmoid(x)
def _layernorm(x, axis=-1, eps=1e-5):
    m = x.mean(dim=axis, keepdim=True)
    v = x.var(dim=axis, keepdim=True, unbiased=False)
    return (x - m) / torch.sqrt(v + eps)
def _scatter_add(tgt, idx, src, axis=0):
    return tgt.clone().index_add_(axis, idx, src)
def _segment_sum(src, seg, n):
    out = torch.zeros((n,) + src.shape[1:], dtype=src.dtype)
    return out.index_add_(0, seg, src)
def _pad(x, pw):
    flat = tuple(v for pair in reversed(pw) for v in pair)
    return torch.nn.functional.pad(x, flat)

kernel_name = 'gated_mix__grouped_kv_mqa__grouped_kv_mqa__prompt'

def setup_inputs():

    _w = (lambda a: (lambda t: t.to(torch.bfloat16) if t.is_floating_point() else t)(torch.as_tensor(a)))
    def _A_setup():
        B, S, D, H = 1, 2048, 512, 32
        Dh = D // H
        return {
            "x": _w((lambda *s: torch.randn(*s, dtype=torch.bfloat16) * 0.05)(B, S, D)),
            "wq": _w((lambda *s: torch.randn(*s, dtype=torch.bfloat16) * 0.8)(D, H * Dh)),
            "wk": _w((lambda *s: torch.randn(*s, dtype=torch.bfloat16) * 0.8)(D, Dh)),
            "wv": _w((lambda *s: torch.randn(*s, dtype=torch.bfloat16) * 0.05)(D, Dh)),
            "wo": _w((lambda *s: torch.randn(*s, dtype=torch.bfloat16) * 0.05)(H * Dh, D)),
        }
    _A_in = _A_setup()

    def _B_setup():
        B, S, D, H = 1, 2048, 512, 32
        Dh = D // H
        return {
            "x": _w((lambda *s: torch.randn(*s, dtype=torch.bfloat16) * 0.05)(B, S, D)),
            "wq": _w((lambda *s: torch.randn(*s, dtype=torch.bfloat16) * 0.8)(D, H * Dh)),
            "wk": _w((lambda *s: torch.randn(*s, dtype=torch.bfloat16) * 0.8)(D, Dh)),
            "wv": _w((lambda *s: torch.randn(*s, dtype=torch.bfloat16) * 0.05)(D, Dh)),
            "wo": _w((lambda *s: torch.randn(*s, dtype=torch.bfloat16) * 0.05)(H * Dh, D)),
        }
    _B_in = _B_setup()

    return {
        "A_x": _A_in["x"],
        "A_wq": _A_in["wq"],
        "A_wk": _A_in["wk"],
        "A_wv": _A_in["wv"],
        "A_wo": _A_in["wo"],
        "B_x": _B_in["x"],
        "B_wq": _B_in["wq"],
        "B_wk": _B_in["wk"],
        "B_wv": _B_in["wv"],
        "B_wo": _B_in["wo"],
        "gate_w": _w((lambda *s: torch.randn(*s, dtype=torch.bfloat16) * 0.05)(512) * 16.0),
    }

def reference(A_x, A_wq, A_wk, A_wv, A_wo, B_x, B_wq, B_wk, B_wv, B_wo, gate_w):
    def _A_ref(x, wq, wk, wv, wo):
        B, S, D = x.shape
        H = 32
        Dh = D // H
        q = (lambda x, *d: x.permute(*d))((x @ wq).reshape(B, S, H, Dh), 0, 2, 1, 3)
        k = x @ wk
        v = x @ wv
        scores = torch.einsum("bhqd,bkd->bhqk", q, k) * (Dh ** -0.5)
        p = _softmax(scores, axis=-1)
        o = torch.einsum("bhqk,bkd->bhqd", p, v)
        return (lambda x, *d: x.permute(*d))(o, 0, 2, 1, 3).reshape(B, S, H * Dh) @ wo

    def _B_ref(x, wq, wk, wv, wo):
        B, S, D = x.shape
        H = 32
        Dh = D // H
        q = (lambda x, *d: x.permute(*d))((x @ wq).reshape(B, S, H, Dh), 0, 2, 1, 3)
        k = x @ wk
        v = x @ wv
        scores = torch.einsum("bhqd,bkd->bhqk", q, k) * (Dh ** -0.5)
        p = _softmax(scores, axis=-1)
        o = torch.einsum("bhqk,bkd->bhqd", p, v)
        return (lambda x, *d: x.permute(*d))(o, 0, 2, 1, 3).reshape(B, S, H * Dh) @ wo

    x = A_x
    g = _sigmoid((x * gate_w).sum(-1, keepdims=True))
    a = _A_ref(x, A_wq, A_wk, A_wv, A_wo)
    b = _B_ref(x, B_wq, B_wk, B_wv, B_wo)
    return g * a + (1 - g) * b
```

```cpp
#include <hip/hip_runtime.h>
#include <hip/hip_bf16.h>
#include <stdint.h>

namespace {

typedef __attribute__((ext_vector_type(16))) float f32x16;
typedef __attribute__((ext_vector_type(4))) float f32x4;
typedef __attribute__((ext_vector_type(8))) __bf16 bf16x8;
typedef __attribute__((ext_vector_type(4))) unsigned u32x4;
typedef __attribute__((ext_vector_type(2))) unsigned u32x2;
#define LAS __attribute__((address_space(3)))
#define GAS __attribute__((address_space(1)))

constexpr int NTHR = 512;

constexpr unsigned WS_EPOCH = 0;
constexpr unsigned KVF_PER_LINE = 8;
constexpr unsigned KVF_BRANCH = 128 / KVF_PER_LINE * 128;
constexpr unsigned WS_KVF = 4096;
__host__ __device__ constexpr unsigned kvf_off(unsigned i) { return i / KVF_PER_LINE * 128 + i % KVF_PER_LINE * 4; }
constexpr unsigned WS_OF = 8192;
constexpr unsigned WS_KV = 65536;
constexpr unsigned WS_G = WS_KV + 2 * 131072;
constexpr unsigned WS_O = WS_G + 16384;

constexpr unsigned L_X = 0, L_WQ = 65536, L_WKV = 131072;
constexpr unsigned L_K = 0, L_V = 65536, L_ONE = 131072, L_REDO = L_ONE + 32, L_SCR = 139264, L_OWNK = 157696, L_TOUCH = 158720;
__host__ __device__ constexpr unsigned part_base(unsigned k) { return k == 0 ? 57344u : k == 3 ? 155648u : 122880u + (k - 1) * 8192u; }
constexpr unsigned LDS_BYTES = 163840;

__device__ __forceinline__ void glds16(const void* sbase, unsigned voff, unsigned lds_dst) {
    unsigned keep;
    asm volatile("s_mov_b32 %0, m0\n\ts_mov_b32 m0, %3\n\ts_nop 0\n\tglobal_load_lds_dwordx4 %1, %2\n\ts_mov_b32 m0, %0"
                 : "=&s"(keep) : "v"(voff), "s"(sbase), "s"(lds_dst) : "memory");
}
__device__ __forceinline__ void glds16_sc1(const void* sbase, unsigned voff, unsigned lds_dst) {
    unsigned keep;
    asm volatile("s_mov_b32 %0, m0\n\ts_mov_b32 m0, %3\n\ts_nop 0\n\tglobal_load_lds_dwordx4 %1, %2 sc1\n\ts_mov_b32 m0, %0"
                 : "=&s"(keep) : "v"(voff), "s"(sbase), "s"(lds_dst) : "memory");
}
__device__ __forceinline__ void glds4_sc1(const void* sbase, unsigned voff, unsigned lds_dst) {
    unsigned keep;
    asm volatile("s_mov_b32 %0, m0\n\ts_mov_b32 m0, %3\n\ts_nop 0\n\tglobal_load_lds_dword %1, %2 sc1\n\ts_mov_b32 m0, %0"
                 : "=&s"(keep) : "v"(voff), "s"(sbase), "s"(lds_dst) : "memory");
}
__device__ __forceinline__ void store16_sc1(const void* sbase, unsigned voff, u32x4 v) {
    asm volatile("global_store_dwordx4 %0, %1, %2 sc1\n\ts_nop 1" :: "v"(voff), "v"(v), "s"(sbase) : "memory");
}
__device__ __forceinline__ void store8_sc1(const void* sbase, unsigned voff, u32x2 v) {
    asm volatile("global_store_dwordx2 %0, %1, %2 sc1" :: "v"(voff), "v"(v), "s"(sbase) : "memory");
}
__device__ __forceinline__ void store4_sc1(const void* sbase, unsigned voff, unsigned v) {
    asm volatile("global_store_dword %0, %1, %2 sc1" :: "v"(voff), "v"(v), "s"(sbase) : "memory");
}
constexpr unsigned SPIN_CAP = 1u << 18;
__device__ __forceinline__ void poll2(const void* sbase, unsigned voff, unsigned tag) {
    for (unsigned spins = 0; spins < SPIN_CAP; ++spins) {
        u32x2 v;
        asm volatile("global_load_dwordx2 %0, %1, %2 sc1\n\ts_waitcnt vmcnt(0)" : "=&v"(v) : "v"(voff), "s"(sbase) : "memory");
        if (__all(v[0] == tag && v[1] == tag)) break;
    }
}
__device__ __forceinline__ void poll1(const void* sbase, unsigned voff, unsigned tag) {
    for (unsigned spins = 0; spins < SPIN_CAP; ++spins) {
        unsigned v;
        asm volatile("global_load_dword %0, %1, %2 sc1\n\ts_waitcnt vmcnt(0)" : "=&v"(v) : "v"(voff), "s"(sbase) : "memory");
        if (__all(v == tag)) break;
    }
}
#define WAIT_VM_BAR(N) asm volatile("s_waitcnt vmcnt(" #N ") lgkmcnt(0)\n\ts_barrier" ::: "memory")

__device__ __forceinline__ u32x4 lds_read16(unsigned a) { return *(const LAS u32x4*)(size_t)a; }
__device__ __forceinline__ bf16x8 as_bf(u32x4 v) { return __builtin_bit_cast(bf16x8, v); }
__device__ __forceinline__ unsigned pack2(float lo, float hi) {
    typedef __attribute__((ext_vector_type(2))) __bf16 bf16x2;
    const bf16x2 v = {(__bf16)lo, (__bf16)hi};
    return __builtin_bit_cast(unsigned, v);
}
#define MFMA32(a, b, c) __builtin_amdgcn_mfma_f32_32x32x16_bf16(a, b, c, 0, 0, 0)
#define MFMA16(a, b, c) __builtin_amdgcn_mfma_f32_16x16x32_bf16(a, b, c, 0, 0, 0)

__device__ __forceinline__ float half_swap_max(float m) {
    const auto r = __builtin_amdgcn_permlane32_swap(__float_as_uint(m), __float_as_uint(m), false, false);
    return fmaxf(__uint_as_float(r[0]), __uint_as_float(r[1]));
}
__device__ __forceinline__ float max16(const f32x16& s) {
    float a = fmaxf(fmaxf(s[0], s[1]), s[2]), b = fmaxf(fmaxf(s[3], s[4]), s[5]);
    a = fmaxf(fmaxf(a, s[6]), s[7]); b = fmaxf(fmaxf(b, s[8]), s[9]);
    a = fmaxf(fmaxf(a, s[10]), s[11]); b = fmaxf(fmaxf(b, s[12]), s[13]);
    return fmaxf(fmaxf(a, s[14]), fmaxf(b, s[15]));
}

struct Ids { int lane, w, rb, sub, b, hg; };
__device__ __forceinline__ Ids make_ids() {
    Ids d; d.lane = threadIdx.x & 63; d.w = __builtin_amdgcn_readfirstlane(threadIdx.x >> 6);
    const int id = blockIdx.x, grp = id & 7, slot = id >> 3;
    d.b = grp & 1; d.hg = slot & 3; d.rb = (grp >> 1) * 8 + (slot >> 2); d.sub = d.b * 4 + d.hg;
    return d;
}

__device__ __forceinline__ unsigned x_lane_off(unsigned l, unsigned odd_group) {
    return (l >> 3) * 1024 + (((l & 7) ^ ((4 * odd_group + (l >> 4)) & 7)) << 4);
}
__device__ __forceinline__ void dma_lo(const Ids& d, unsigned lds, const char* x, const char* wk, const char* wv) {
    const unsigned l = d.lane, q = d.w & 3;
    asm volatile(
        "s_mov_b32 s44, m0\n"
        "s_mov_b64 s[42:43], %2\n"
        "s_mov_b32 s45, %3\n"
        "s_mov_b32 m0, s45\n"
        "s_add_u32 s45, s45, 1024\n"
        "global_load_lds_dwordx4 %0, s[42:43]\n"
        "s_add_u32 s42, s42, 8192\n"
        "s_addc_u32 s43, s43, 0\n"
        "s_mov_b32 m0, s45\n"
        "s_add_u32 s45, s45, 7168\n"
        "global_load_lds_dwordx4 %1, s[42:43]\n"
        "s_sub_u32 s42, s42, 8064\n"
        "s_subb_u32 s43, s43, 0\n"
        "s_mov_b32 m0, s45\n"
        "s_add_u32 s45, s45, 1024\n"
        "global_load_lds_dwordx4 %0, s[42:43]\n"
        "s_add_u32 s42, s42, 8192\n"
        "s_addc_u32 s43, s43, 0\n"
        "s_mov_b32 m0, s45\n"
        "s_add_u32 s45, s45, 1024\n"
        "global_load_lds_dwordx4 %1, s[42:43]\n"
        "s_mov_b64 s[42:43], %5\n"
        "s_mov_b32 s45, %7\n"
        "s_mov_b32 m0, s45\n"
        "s_add_u32 s45, s45, 1024\n"
        "global_load_lds_dwordx4 %4, s[42:43]\n"
        "s_add_u32 s42, s42, 1024\n"
        "s_addc_u32 s43, s43, 0\n"
        "s_mov_b32 m0, s45\n"
        "s_add_u32 s45, s45, 1024\n"
        "global_load_lds_dwordx4 %4, s[42:43]\n"
        "s_add_u32 s42, s42, 1024\n"
        "s_addc_u32 s43, s43, 0\n"
        "s_mov_b32 m0, s45\n"
        "s_add_u32 s45, s45, 1024\n"
        "global_load_lds_dwordx4 %4, s[42:43]\n"
        "s_add_u32 s42, s42, 1024\n"
        "s_addc_u32 s43, s43, 0\n"
        "s_mov_b32 m0, s45\n"
        "s_add_u32 s45, s45, 1024\n"
        "global_load_lds_dwordx4 %4, s[42:43]\n"
        "s_mov_b64 s[42:43], %6\n"
        "s_add_u32 s45, s45, 12288\n"
        "s_mov_b32 m0, s45\n"
        "s_add_u32 s45, s45, 1024\n"
        "global_load_lds_dwordx4 %4, s[42:43]\n"
        "s_add_u32 s42, s42, 1024\n"
        "s_addc_u32 s43, s43, 0\n"
        "s_mov_b32 m0, s45\n"
        "s_add_u32 s45, s45, 1024\n"
        "global_load_lds_dwordx4 %4, s[42:43]\n"
        "s_add_u32 s42, s42, 1024\n"
        "s_addc_u32 s43, s43, 0\n"
        "s_mov_b32 m0, s45\n"
        "s_add_u32 s45, s45, 1024\n"
        "global_load_lds_dwordx4 %4, s[42:43]\n"
        "s_add_u32 s42, s42, 1024\n"
        "s_addc_u32 s43, s43, 0\n"
        "s_mov_b32 m0, s45\n"
        "s_add_u32 s45, s45, 1024\n"
        "global_load_lds_dwordx4 %4, s[42:43]\n"
        "s_mov_b32 m0, s44\n"
        :
        : "v"(x_lane_off(l, 0)), "v"(x_lane_off(l, 1)), "s"(x + (size_t)(d.rb * 64 + d.hg * 16) * 1024 + q * 256), "s"(lds + L_X + q * 16384 + d.hg * 2048), "v"((l ^ (((l >> 4) & 1) << 3)) * 16), "s"(wk + q * 4096), "s"(wv + q * 4096), "s"(lds + L_WKV + q * 4096)
        : "s42", "s43", "s44", "s45", "scc", "memory");
}

__device__ __forceinline__ void phase_kv(const Ids& d, unsigned lds, char* ws, u32x2& vk) {
    const int l = d.lane, row = l & 15, kg = l >> 4, q = (l & 15) >> 2, p = l & 3;
    const unsigned xb = lds + L_X + (16 * d.hg + row) * 128 + ((kg ^ (row >> 1)) << 4);
    const int r0 = d.rb * 64 + d.hg * 16;
    const unsigned wo = ((((8 * kg + q) * 2 + (p >> 1)) ^ ((kg & 1) << 3)) << 4) + 8 * (p & 1);
    u32x2 vv;
    asm volatile(
        "ds_read_b128 v[208:211], %4 offset:0\n"
        "ds_read_b64_tr_b16 v[212:213], %6 offset:0\n"
        "ds_read_b64_tr_b16 v[214:215], %7 offset:0\n"
        "ds_read_b64_tr_b16 v[216:217], %6 offset:16384\n"
        "ds_read_b64_tr_b16 v[218:219], %7 offset:16384\n"
        "ds_read_b128 v[220:223], %5 offset:0\n"
        "ds_read_b64_tr_b16 v[224:225], %6 offset:1024\n"
        "ds_read_b64_tr_b16 v[226:227], %7 offset:1024\n"
        "ds_read_b64_tr_b16 v[228:229], %6 offset:17408\n"
        "ds_read_b64_tr_b16 v[230:231], %7 offset:17408\n"
        "ds_read_b128 v[232:235], %4 offset:8192\n"
        "ds_read_b64_tr_b16 v[236:237], %6 offset:2048\n"
        "ds_read_b64_tr_b16 v[238:239], %7 offset:2048\n"
        "ds_read_b64_tr_b16 v[240:241], %6 offset:18432\n"
        "ds_read_b64_tr_b16 v[242:243], %7 offset:18432\n"
        "s_waitcnt lgkmcnt(10)\n"
        "v_mfma_f32_16x16x32_bf16 v[244:247], v[212:215], v[208:211], 0\n"
        "v_mfma_f32_16x16x32_bf16 v[248:251], v[208:211], v[216:219], 0\n"
        "ds_read_b128 v[208:211], %5 offset:8192\n"
        "ds_read_b64_tr_b16 v[212:213], %6 offset:3072\n"
        "ds_read_b64_tr_b16 v[214:215], %7 offset:3072\n"
        "ds_read_b64_tr_b16 v[216:217], %6 offset:19456\n"
        "ds_read_b64_tr_b16 v[218:219], %7 offset:19456\n"
        "s_waitcnt lgkmcnt(10)\n"
        "v_mfma_f32_16x16x32_bf16 v[244:247], v[224:227], v[220:223], v[244:247]\n"
        "v_mfma_f32_16x16x32_bf16 v[248:251], v[220:223], v[228:231], v[248:251]\n"
        "ds_read_b128 v[220:223], %4 offset:16384\n"
        "ds_read_b64_tr_b16 v[224:225], %6 offset:4096\n"
        "ds_read_b64_tr_b16 v[226:227], %7 offset:4096\n"
        "ds_read_b64_tr_b16 v[228:229], %6 offset:20480\n"
        "ds_read_b64_tr_b16 v[230:231], %7 offset:20480\n"
        "s_waitcnt lgkmcnt(10)\n"
        "v_mfma_f32_16x16x32_bf16 v[244:247], v[236:239], v[232:235], v[244:247]\n"
        "v_mfma_f32_16x16x32_bf16 v[248:251], v[232:235], v[240:243], v[248:251]\n"
        "ds_read_b128 v[232:235], %5 offset:16384\n"
        "ds_read_b64_tr_b16 v[236:237], %6 offset:5120\n"
        "ds_read_b64_tr_b16 v[238:239], %7 offset:5120\n"
        "ds_read_b64_tr_b16 v[240:241], %6 offset:21504\n"
        "ds_read_b64_tr_b16 v[242:243], %7 offset:21504\n"
        "s_waitcnt lgkmcnt(10)\n"
        "v_mfma_f32_16x16x32_bf16 v[244:247], v[212:215], v[208:211], v[244:247]\n"
        "v_mfma_f32_16x16x32_bf16 v[248:251], v[208:211], v[216:219], v[248:251]\n"
        "ds_read_b128 v[208:211], %4 offset:24576\n"
        "ds_read_b64_tr_b16 v[212:213], %6 offset:6144\n"
        "ds_read_b64_tr_b16 v[214:215], %7 offset:6144\n"
        "ds_read_b64_tr_b16 v[216:217], %6 offset:22528\n"
        "ds_read_b64_tr_b16 v[218:219], %7 offset:22528\n"
        "s_waitcnt lgkmcnt(10)\n"
        "v_mfma_f32_16x16x32_bf16 v[244:247], v[224:227], v[220:223], v[244:247]\n"
        "v_mfma_f32_16x16x32_bf16 v[248:251], v[220:223], v[228:231], v[248:251]\n"
        "ds_read_b128 v[220:223], %5 offset:24576\n"
        "ds_read_b64_tr_b16 v[224:225], %6 offset:7168\n"
        "ds_read_b64_tr_b16 v[226:227], %7 offset:7168\n"
        "ds_read_b64_tr_b16 v[228:229], %6 offset:23552\n"
        "ds_read_b64_tr_b16 v[230:231], %7 offset:23552\n"
        "s_waitcnt lgkmcnt(10)\n"
        "v_mfma_f32_16x16x32_bf16 v[244:247], v[236:239], v[232:235], v[244:247]\n"
        "v_mfma_f32_16x16x32_bf16 v[248:251], v[232:235], v[240:243], v[248:251]\n"
        "ds_read_b128 v[232:235], %4 offset:32768\n"
        "ds_read_b64_tr_b16 v[236:237], %6 offset:8192\n"
        "ds_read_b64_tr_b16 v[238:239], %7 offset:8192\n"
        "ds_read_b64_tr_b16 v[240:241], %6 offset:24576\n"
        "ds_read_b64_tr_b16 v[242:243], %7 offset:24576\n"
        "s_waitcnt lgkmcnt(10)\n"
        "v_mfma_f32_16x16x32_bf16 v[244:247], v[212:215], v[208:211], v[244:247]\n"
        "v_mfma_f32_16x16x32_bf16 v[248:251], v[208:211], v[216:219], v[248:251]\n"
        "ds_read_b128 v[208:211], %5 offset:32768\n"
        "ds_read_b64_tr_b16 v[212:213], %6 offset:9216\n"
        "ds_read_b64_tr_b16 v[214:215], %7 offset:9216\n"
        "ds_read_b64_tr_b16 v[216:217], %6 offset:25600\n"
        "ds_read_b64_tr_b16 v[218:219], %7 offset:25600\n"
        "s_waitcnt lgkmcnt(10)\n"
        "v_mfma_f32_16x16x32_bf16 v[244:247], v[224:227], v[220:223], v[244:247]\n"
        "v_mfma_f32_16x16x32_bf16 v[248:251], v[220:223], v[228:231], v[248:251]\n"
        "ds_read_b128 v[220:223], %4 offset:40960\n"
        "ds_read_b64_tr_b16 v[224:225], %6 offset:10240\n"
        "ds_read_b64_tr_b16 v[226:227], %7 offset:10240\n"
        "ds_read_b64_tr_b16 v[228:229], %6 offset:26624\n"
        "ds_read_b64_tr_b16 v[230:231], %7 offset:26624\n"
        "s_waitcnt lgkmcnt(10)\n"
        "v_mfma_f32_16x16x32_bf16 v[244:247], v[236:239], v[232:235], v[244:247]\n"
        "v_mfma_f32_16x16x32_bf16 v[248:251], v[232:235], v[240:243], v[248:251]\n"
        "ds_read_b128 v[232:235], %5 offset:40960\n"
        "ds_read_b64_tr_b16 v[236:237], %6 offset:11264\n"
        "ds_read_b64_tr_b16 v[238:239], %7 offset:11264\n"
        "ds_read_b64_tr_b16 v[240:241], %6 offset:27648\n"
        "ds_read_b64_tr_b16 v[242:243], %7 offset:27648\n"
        "s_waitcnt lgkmcnt(10)\n"
        "v_mfma_f32_16x16x32_bf16 v[244:247], v[212:215], v[208:211], v[244:247]\n"
        "v_mfma_f32_16x16x32_bf16 v[248:251], v[208:211], v[216:219], v[248:251]\n"
        "ds_read_b128 v[208:211], %4 offset:49152\n"
        "ds_read_b64_tr_b16 v[212:213], %6 offset:12288\n"
        "ds_read_b64_tr_b16 v[214:215], %7 offset:12288\n"
        "ds_read_b64_tr_b16 v[216:217], %6 offset:28672\n"
        "ds_read_b64_tr_b16 v[218:219], %7 offset:28672\n"
        "s_waitcnt lgkmcnt(10)\n"
        "v_mfma_f32_16x16x32_bf16 v[244:247], v[224:227], v[220:223], v[244:247]\n"
        "v_mfma_f32_16x16x32_bf16 v[248:251], v[220:223], v[228:231], v[248:251]\n"
        "ds_read_b128 v[220:223], %5 offset:49152\n"
        "ds_read_b64_tr_b16 v[224:225], %6 offset:13312\n"
        "ds_read_b64_tr_b16 v[226:227], %7 offset:13312\n"
        "ds_read_b64_tr_b16 v[228:229], %6 offset:29696\n"
        "ds_read_b64_tr_b16 v[230:231], %7 offset:29696\n"
        "s_waitcnt lgkmcnt(10)\n"
        "v_mfma_f32_16x16x32_bf16 v[244:247], v[236:239], v[232:235], v[244:247]\n"
        "v_mfma_f32_16x16x32_bf16 v[248:251], v[232:235], v[240:243], v[248:251]\n"
        "ds_read_b128 v[232:235], %4 offset:57344\n"
        "ds_read_b64_tr_b16 v[236:237], %6 offset:14336\n"
        "ds_read_b64_tr_b16 v[238:239], %7 offset:14336\n"
        "ds_read_b64_tr_b16 v[240:241], %6 offset:30720\n"
        "ds_read_b64_tr_b16 v[242:243], %7 offset:30720\n"
        "s_waitcnt lgkmcnt(10)\n"
        "v_mfma_f32_16x16x32_bf16 v[244:247], v[212:215], v[208:211], v[244:247]\n"
        "v_mfma_f32_16x16x32_bf16 v[248:251], v[208:211], v[216:219], v[248:251]\n"
        "ds_read_b128 v[208:211], %5 offset:57344\n"
        "ds_read_b64_tr_b16 v[212:213], %6 offset:15360\n"
        "ds_read_b64_tr_b16 v[214:215], %7 offset:15360\n"
        "ds_read_b64_tr_b16 v[216:217], %6 offset:31744\n"
        "ds_read_b64_tr_b16 v[218:219], %7 offset:31744\n"
        "s_waitcnt lgkmcnt(10)\n"
        "v_mfma_f32_16x16x32_bf16 v[244:247], v[224:227], v[220:223], v[244:247]\n"
        "v_mfma_f32_16x16x32_bf16 v[248:251], v[220:223], v[228:231], v[248:251]\n"
        "s_waitcnt lgkmcnt(5)\n"
        "v_mfma_f32_16x16x32_bf16 v[244:247], v[236:239], v[232:235], v[244:247]\n"
        "v_mfma_f32_16x16x32_bf16 v[248:251], v[232:235], v[240:243], v[248:251]\n"
        "s_waitcnt lgkmcnt(0)\n"
        "v_mfma_f32_16x16x32_bf16 v[244:247], v[212:215], v[208:211], v[244:247]\n"
        "v_mfma_f32_16x16x32_bf16 v[248:251], v[208:211], v[216:219], v[248:251]\n"
        "s_nop 7\n"
        "s_nop 1\n"
        "v_cvt_pk_bf16_f32 %0, v244, v245\n"
        "v_cvt_pk_bf16_f32 %1, v246, v247\n"
        "v_cvt_pk_bf16_f32 %2, v248, v249\n"
        "v_cvt_pk_bf16_f32 %3, v250, v251\n"
        : "=&v"(vk[0]), "=&v"(vk[1]), "=&v"(vv[0]), "=&v"(vv[1])
        : "v"(xb), "v"(xb ^ 64), "v"(lds + L_WKV + wo), "v"(lds + L_WKV + (wo ^ 128))
        : "v208", "v209", "v210", "v211", "v212", "v213", "v214", "v215", "v216", "v217", "v218", "v219", "v220", "v221", "v222", "v223", "v224", "v225", "v226", "v227", "v228", "v229", "v230", "v231", "v232", "v233", "v234", "v235", "v236", "v237", "v238", "v239", "v240", "v241", "v242", "v243", "v244", "v245", "v246", "v247", "v248", "v249", "v250", "v251", "memory");
    const char* kv = ws + WS_KV + d.b * 131072;
    store8_sc1(kv, (r0 >> 5) * 1024 + ((kg & 1) * 32 + (r0 & 31) + row) * 16 + (kg >> 1) * 8, vk);
    store8_sc1(kv, 65536 + (r0 >> 4) * 512 + (kg >> 1) * 256 + row * 16 + (kg & 1) * 8, vv);
}
__device__ __forceinline__ void phase_gate(const Ids& d, unsigned lds, char* ws, const u32x4 (&gw)[16]) {
    const int l = d.lane, row = l & 15, kg = l >> 4;
    const unsigned xb = lds + L_X + (16 * d.hg + row) * 128 + ((kg ^ (row >> 1)) << 4);
    const int r0 = d.rb * 64 + d.hg * 16;
    f32x4 acc = {0.f, 0.f, 0.f, 0.f};
#pragma unroll
    for (int s = 0; s < 16; ++s) {
        const bf16x8 xf = as_bf(lds_read16((xb ^ ((s & 1) << 6)) + (s >> 1) * 8192));
        acc = MFMA16(xf, as_bf(gw[s]), acc);
    }
    u32x4 g;
#pragma unroll
    for (int i = 0; i < 4; ++i) g[i] = __float_as_uint(1.f / (1.f + __expf(-acc[i])));
    if (row == 0) store16_sc1(ws + WS_G + d.b * 8192, (r0 + 4 * kg) * 4, g);
}

__device__ __forceinline__ void phase_q(const Ids& d, unsigned lds, const char* x, const char* wq, char* ws, unsigned& epoch,
                                        u32x4& qa, u32x4& qb, u32x2& flags) {
    const int l = d.lane, h = l >> 5, g1 = (l >> 4) & 1, q4 = (l & 15) >> 2, p = l & 3;
    const unsigned rg = d.w & 1, hp = d.w >> 1, q = d.w & 3;
    const unsigned xb = lds + L_X + (rg * 32 + (l & 31)) * 128 + ((h ^ ((l & 15) >> 1)) << 4);
    unsigned wb = lds + L_WQ + (8 * h + q4) * 256 + (((4 * hp + 2 * g1 + (p >> 1)) ^ (q4 << 2)) << 4) + 8 * (p & 1);
    const unsigned row = l >> 4, sc = l & 15;
    const unsigned voff = row * 1024 + (sc ^ (row << 2)) * 16;
    const char* wq0 = wq + (size_t)(q * 16) * 1024 + d.hg * 256;
    const unsigned ring = lds + L_WQ + q * 4096;
    const int role = d.w == 0 ? 0 : d.w < 4 ? 1 : 2;
    unsigned xp[3];
#pragma unroll
    for (int i = 0; i < 3; ++i) {
        const unsigned n = 3 * q + i, second = n >= 6, j = n - 6 * second, g = j + (j >= 2 * (unsigned)d.hg ? 2 : 0);
        xp[i] = g * 8192 + second * 128;
    }
    asm volatile(
        "s_mov_b32 s44, m0\n"
        "s_cmp_lt_u32 %17, 2\n"
        "s_cbranch_scc1 9f\n"
        "s_mov_b64 s[42:43], %15\n"
        "s_mov_b32 s45, %16\n"
        "s_mov_b64 s[46:47], %22\n"
        "s_add_u32 s46, s46, %26\n"
        "s_addc_u32 s47, s47, 0\n"
        "s_lshr_b32 s55, %26, 13\n"
        "s_lshl_b32 s55, s55, 10\n"
        "s_and_b32 s56, %26, 128\n"
        "s_lshl_b32 s56, s56, 6\n"
        "s_add_u32 s55, s55, s56\n"
        "s_add_u32 s52, s55, %23\n"
        "s_mov_b64 s[48:49], %22\n"
        "s_add_u32 s48, s48, %27\n"
        "s_addc_u32 s49, s49, 0\n"
        "s_lshr_b32 s55, %27, 13\n"
        "s_lshl_b32 s55, s55, 10\n"
        "s_and_b32 s56, %27, 128\n"
        "s_lshl_b32 s56, s56, 6\n"
        "s_add_u32 s55, s55, s56\n"
        "s_add_u32 s53, s55, %23\n"
        "s_mov_b64 s[50:51], %22\n"
        "s_add_u32 s50, s50, %28\n"
        "s_addc_u32 s51, s51, 0\n"
        "s_lshr_b32 s55, %28, 13\n"
        "s_lshl_b32 s55, s55, 10\n"
        "s_and_b32 s56, %28, 128\n"
        "s_lshl_b32 s56, s56, 6\n"
        "s_add_u32 s55, s55, s56\n"
        "s_add_u32 s54, s55, %23\n"
        "s_mov_b32 m0, s52\n"
        "s_add_u32 s52, s52, 16384\n"
        "global_load_lds_dwordx4 %24, s[46:47]\n"
        "s_add_u32 s46, s46, 256\n"
        "s_addc_u32 s47, s47, 0\n"
        "s_mov_b32 m0, s53\n"
        "s_add_u32 s53, s53, 16384\n"
        "global_load_lds_dwordx4 %25, s[48:49]\n"
        "s_add_u32 s48, s48, 256\n"
        "s_addc_u32 s49, s49, 0\n"
        "s_mov_b32 m0, s54\n"
        "s_add_u32 s54, s54, 16384\n"
        "global_load_lds_dwordx4 %24, s[50:51]\n"
        "s_add_u32 s50, s50, 256\n"
        "s_addc_u32 s51, s51, 0\n"
        "s_cmp_eq_u64 %29, 0\n"
        "s_cbranch_scc1 7f\n"
        "v_mbcnt_lo_u32_b32 v254, -1, 0\n"
        "v_mbcnt_hi_u32_b32 v254, -1, v254\n"
        "v_lshlrev_b32 v254, 6, v254\n"
        "global_load_dword v255, v254, %29\n"
        "v_add_u32 v254, 0x1000, v254\n"
        "global_load_dword v255, v254, %29\n"
        "7:\n"
        "s_barrier\n"
        "s_mov_b32 m0, s45\n"
        "s_add_u32 s45, s45, 1024\n"
        "global_load_lds_dwordx4 %14, s[42:43]\n"
        "s_add_u32 s42, s42, 4096\n"
        "s_addc_u32 s43, s43, 0\n"
        "s_mov_b32 m0, s45\n"
        "s_add_u32 s45, s45, 1024\n"
        "global_load_lds_dwordx4 %14, s[42:43]\n"
        "s_add_u32 s42, s42, 4096\n"
        "s_addc_u32 s43, s43, 0\n"
        "s_mov_b32 m0, s45\n"
        "s_add_u32 s45, s45, 1024\n"
        "global_load_lds_dwordx4 %14, s[42:43]\n"
        "s_add_u32 s42, s42, 4096\n"
        "s_addc_u32 s43, s43, 0\n"
        "s_mov_b32 m0, s45\n"
        "s_add_u32 s45, s45, 1024\n"
        "global_load_lds_dwordx4 %14, s[42:43]\n"
        "s_add_u32 s42, s42, 4096\n"
        "s_addc_u32 s43, s43, 0\n"
        "s_add_u32 s42, s42, 49152\n"
        "s_addc_u32 s43, s43, 0\n"
        "s_add_u32 s45, s45, 12288\n"
        "s_mov_b32 m0, s45\n"
        "s_add_u32 s45, s45, 1024\n"
        "global_load_lds_dwordx4 %14, s[42:43]\n"
        "s_add_u32 s42, s42, 4096\n"
        "s_addc_u32 s43, s43, 0\n"
        "s_mov_b32 m0, s45\n"
        "s_add_u32 s45, s45, 1024\n"
        "global_load_lds_dwordx4 %14, s[42:43]\n"
        "s_add_u32 s42, s42, 4096\n"
        "s_addc_u32 s43, s43, 0\n"
        "s_mov_b32 m0, s45\n"
        "s_add_u32 s45, s45, 1024\n"
        "global_load_lds_dwordx4 %14, s[42:43]\n"
        "s_add_u32 s42, s42, 4096\n"
        "s_addc_u32 s43, s43, 0\n"
        "s_mov_b32 m0, s45\n"
        "s_add_u32 s45, s45, 1024\n"
        "global_load_lds_dwordx4 %14, s[42:43]\n"
        "s_add_u32 s42, s42, 4096\n"
        "s_addc_u32 s43, s43, 0\n"
        "s_add_u32 s42, s42, 49152\n"
        "s_addc_u32 s43, s43, 0\n"
        "s_add_u32 s45, s45, 12288\n"
        "s_mov_b32 m0, s52\n"
        "s_add_u32 s52, s52, 16384\n"
        "global_load_lds_dwordx4 %24, s[46:47]\n"
        "s_add_u32 s46, s46, 256\n"
        "s_addc_u32 s47, s47, 0\n"
        "s_mov_b32 m0, s53\n"
        "s_add_u32 s53, s53, 16384\n"
        "global_load_lds_dwordx4 %25, s[48:49]\n"
        "s_add_u32 s48, s48, 256\n"
        "s_addc_u32 s49, s49, 0\n"
        "s_mov_b32 m0, s54\n"
        "s_add_u32 s54, s54, 16384\n"
        "global_load_lds_dwordx4 %24, s[50:51]\n"
        "s_add_u32 s50, s50, 256\n"
        "s_addc_u32 s51, s51, 0\n"
        "s_mov_b32 m0, s45\n"
        "s_add_u32 s45, s45, 1024\n"
        "global_load_lds_dwordx4 %14, s[42:43]\n"
        "s_add_u32 s42, s42, 4096\n"
        "s_addc_u32 s43, s43, 0\n"
        "s_mov_b32 m0, s45\n"
        "s_add_u32 s45, s45, 1024\n"
        "global_load_lds_dwordx4 %14, s[42:43]\n"
        "s_add_u32 s42, s42, 4096\n"
        "s_addc_u32 s43, s43, 0\n"
        "s_mov_b32 m0, s45\n"
        "s_add_u32 s45, s45, 1024\n"
        "global_load_lds_dwordx4 %14, s[42:43]\n"
        "s_add_u32 s42, s42, 4096\n"
        "s_addc_u32 s43, s43, 0\n"
        "s_mov_b32 m0, s45\n"
        "s_add_u32 s45, s45, 1024\n"
        "global_load_lds_dwordx4 %14, s[42:43]\n"
        "s_add_u32 s42, s42, 4096\n"
        "s_addc_u32 s43, s43, 0\n"
        "s_add_u32 s42, s42, 49152\n"
        "s_addc_u32 s43, s43, 0\n"
        "s_add_u32 s45, s45, 12288\n"
        "9:\n"
        "s_cmp_lt_u32 %17, 2\n"
        "s_cbranch_scc1 10f\n"
        "s_waitcnt vmcnt(11) lgkmcnt(0)\n"
        "s_barrier\n"
        "s_mov_b32 m0, s45\n"
        "s_add_u32 s45, s45, 1024\n"
        "global_load_lds_dwordx4 %14, s[42:43]\n"
        "s_add_u32 s42, s42, 4096\n"
        "s_addc_u32 s43, s43, 0\n"
        "s_mov_b32 m0, s45\n"
        "s_add_u32 s45, s45, 1024\n"
        "global_load_lds_dwordx4 %14, s[42:43]\n"
        "s_add_u32 s42, s42, 4096\n"
        "s_addc_u32 s43, s43, 0\n"
        "s_mov_b32 m0, s45\n"
        "s_add_u32 s45, s45, 1024\n"
        "global_load_lds_dwordx4 %14, s[42:43]\n"
        "s_add_u32 s42, s42, 4096\n"
        "s_addc_u32 s43, s43, 0\n"
        "s_mov_b32 m0, s45\n"
        "s_add_u32 s45, s45, 1024\n"
        "global_load_lds_dwordx4 %14, s[42:43]\n"
        "s_add_u32 s42, s42, 4096\n"
        "s_addc_u32 s43, s43, 0\n"
        "s_add_u32 s42, s42, 49152\n"
        "s_addc_u32 s43, s43, 0\n"
        "s_add_u32 s45, s45, 12288\n"
        "s_mov_b32 m0, s45\n"
        "s_add_u32 s45, s45, 1024\n"
        "global_load_lds_dwordx4 %14, s[42:43]\n"
        "s_add_u32 s42, s42, 4096\n"
        "s_addc_u32 s43, s43, 0\n"
        "s_mov_b32 m0, s45\n"
        "s_add_u32 s45, s45, 1024\n"
        "global_load_lds_dwordx4 %14, s[42:43]\n"
        "s_add_u32 s42, s42, 4096\n"
        "s_addc_u32 s43, s43, 0\n"
        "s_mov_b32 m0, s45\n"
        "s_add_u32 s45, s45, 1024\n"
        "global_load_lds_dwordx4 %14, s[42:43]\n"
        "s_add_u32 s42, s42, 4096\n"
        "s_addc_u32 s43, s43, 0\n"
        "s_mov_b32 m0, s45\n"
        "s_add_u32 s45, s45, 1024\n"
        "global_load_lds_dwordx4 %14, s[42:43]\n"
        "s_add_u32 s42, s42, 4096\n"
        "s_addc_u32 s43, s43, 0\n"
        "s_add_u32 s42, s42, 49152\n"
        "s_addc_u32 s43, s43, 0\n"
        "s_add_u32 s45, s45, 12288\n"
        "s_branch 20f\n"
        "10:\n"
        "s_waitcnt lgkmcnt(0)\n"
        "s_barrier\n"
        "20:\n"
        "ds_read_b64_tr_b16 v[176:177], %9 offset:0\n"
        "ds_read_b64_tr_b16 v[178:179], %9 offset:1024\n"
        "ds_read_b128 v[180:183], %10 offset:0\n"
        "ds_read_b64_tr_b16 v[184:185], %9 offset:4096\n"
        "ds_read_b64_tr_b16 v[186:187], %9 offset:5120\n"
        "ds_read_b128 v[188:191], %11 offset:0\n"
        "ds_read_b64_tr_b16 v[192:193], %9 offset:8192\n"
        "ds_read_b64_tr_b16 v[194:195], %9 offset:9216\n"
        "ds_read_b128 v[196:199], %12 offset:0\n"
        "ds_read_b64_tr_b16 v[200:201], %9 offset:12288\n"
        "ds_read_b64_tr_b16 v[202:203], %9 offset:13312\n"
        "ds_read_b128 v[204:207], %13 offset:0\n"
        "s_cmp_lg_u32 %17, 0\n"
        "s_cbranch_scc1 30f\n"
        "s_waitcnt vmcnt(0)\n"
        "v_add_u32 v240, 1, %18\n"
        "s_mov_b64 exec, 1\n"
        "global_store_dword %20, v240, %19 sc1\n"
        "s_mov_b64 exec, -1\n"
        "30:\n"
        "s_cmp_lt_u32 %17, 2\n"
        "s_cbranch_scc1 11f\n"
        "s_waitcnt vmcnt(15) lgkmcnt(0)\n"
        "s_barrier\n"
        "s_mov_b32 m0, s52\n"
        "s_add_u32 s52, s52, 16384\n"
        "global_load_lds_dwordx4 %24, s[46:47]\n"
        "s_add_u32 s46, s46, 256\n"
        "s_addc_u32 s47, s47, 0\n"
        "s_mov_b32 m0, s53\n"
        "s_add_u32 s53, s53, 16384\n"
        "global_load_lds_dwordx4 %25, s[48:49]\n"
        "s_add_u32 s48, s48, 256\n"
        "s_addc_u32 s49, s49, 0\n"
        "s_mov_b32 m0, s54\n"
        "s_add_u32 s54, s54, 16384\n"
        "global_load_lds_dwordx4 %24, s[50:51]\n"
        "s_add_u32 s50, s50, 256\n"
        "s_addc_u32 s51, s51, 0\n"
        "s_mov_b32 m0, s45\n"
        "s_add_u32 s45, s45, 1024\n"
        "global_load_lds_dwordx4 %14, s[42:43]\n"
        "s_add_u32 s42, s42, 4096\n"
        "s_addc_u32 s43, s43, 0\n"
        "s_mov_b32 m0, s45\n"
        "s_add_u32 s45, s45, 1024\n"
        "global_load_lds_dwordx4 %14, s[42:43]\n"
        "s_add_u32 s42, s42, 4096\n"
        "s_addc_u32 s43, s43, 0\n"
        "s_mov_b32 m0, s45\n"
        "s_add_u32 s45, s45, 1024\n"
        "global_load_lds_dwordx4 %14, s[42:43]\n"
        "s_add_u32 s42, s42, 4096\n"
        "s_addc_u32 s43, s43, 0\n"
        "s_mov_b32 m0, s45\n"
        "s_add_u32 s45, s45, 1024\n"
        "global_load_lds_dwordx4 %14, s[42:43]\n"
        "s_add_u32 s42, s42, 4096\n"
        "s_addc_u32 s43, s43, 0\n"
        "s_add_u32 s42, s42, 49152\n"
        "s_addc_u32 s43, s43, 0\n"
        "s_add_u32 s45, s45, 12288\n"
        "s_branch 21f\n"
        "11:\n"
        "s_waitcnt lgkmcnt(0)\n"
        "s_barrier\n"
        "21:\n"
        "v_mfma_f32_32x32x16_bf16 v[240:255], v[176:179], v[180:183], 0\n"
        "ds_read_b64_tr_b16 v[208:209], %9 offset:16384\n"
        "ds_read_b64_tr_b16 v[210:211], %9 offset:17408\n"
        "ds_read_b128 v[212:215], %10 offset:8192\n"
        "v_mfma_f32_32x32x16_bf16 v[240:255], v[184:187], v[188:191], v[240:255]\n"
        "ds_read_b64_tr_b16 v[216:217], %9 offset:20480\n"
        "ds_read_b64_tr_b16 v[218:219], %9 offset:21504\n"
        "ds_read_b128 v[220:223], %11 offset:8192\n"
        "v_mfma_f32_32x32x16_bf16 v[240:255], v[192:195], v[196:199], v[240:255]\n"
        "ds_read_b64_tr_b16 v[224:225], %9 offset:24576\n"
        "ds_read_b64_tr_b16 v[226:227], %9 offset:25600\n"
        "ds_read_b128 v[228:231], %12 offset:8192\n"
        "v_mfma_f32_32x32x16_bf16 v[240:255], v[200:203], v[204:207], v[240:255]\n"
        "ds_read_b64_tr_b16 v[232:233], %9 offset:28672\n"
        "ds_read_b64_tr_b16 v[234:235], %9 offset:29696\n"
        "ds_read_b128 v[236:239], %13 offset:8192\n"
        "s_cmp_lt_u32 %17, 2\n"
        "s_cbranch_scc1 12f\n"
        "s_waitcnt vmcnt(15) lgkmcnt(0)\n"
        "s_barrier\n"
        "s_sub_u32 s45, s45, 98304\n"
        "s_mov_b32 m0, s45\n"
        "s_add_u32 s45, s45, 1024\n"
        "global_load_lds_dwordx4 %14, s[42:43]\n"
        "s_add_u32 s42, s42, 4096\n"
        "s_addc_u32 s43, s43, 0\n"
        "s_mov_b32 m0, s45\n"
        "s_add_u32 s45, s45, 1024\n"
        "global_load_lds_dwordx4 %14, s[42:43]\n"
        "s_add_u32 s42, s42, 4096\n"
        "s_addc_u32 s43, s43, 0\n"
        "s_mov_b32 m0, s45\n"
        "s_add_u32 s45, s45, 1024\n"
        "global_load_lds_dwordx4 %14, s[42:43]\n"
        "s_add_u32 s42, s42, 4096\n"
        "s_addc_u32 s43, s43, 0\n"
        "s_mov_b32 m0, s45\n"
        "s_add_u32 s45, s45, 1024\n"
        "global_load_lds_dwordx4 %14, s[42:43]\n"
        "s_add_u32 s42, s42, 4096\n"
        "s_addc_u32 s43, s43, 0\n"
        "s_add_u32 s42, s42, 49152\n"
        "s_addc_u32 s43, s43, 0\n"
        "s_add_u32 s45, s45, 12288\n"
        "s_mov_b32 m0, s52\n"
        "s_add_u32 s52, s52, 16384\n"
        "global_load_lds_dwordx4 %24, s[46:47]\n"
        "s_add_u32 s46, s46, 256\n"
        "s_addc_u32 s47, s47, 0\n"
        "s_mov_b32 m0, s53\n"
        "s_add_u32 s53, s53, 16384\n"
        "global_load_lds_dwordx4 %25, s[48:49]\n"
        "s_add_u32 s48, s48, 256\n"
        "s_addc_u32 s49, s49, 0\n"
        "s_mov_b32 m0, s54\n"
        "s_add_u32 s54, s54, 16384\n"
        "global_load_lds_dwordx4 %24, s[50:51]\n"
        "s_add_u32 s50, s50, 256\n"
        "s_addc_u32 s51, s51, 0\n"
        "s_branch 22f\n"
        "12:\n"
        "s_waitcnt lgkmcnt(0)\n"
        "s_barrier\n"
        "22:\n"
        "v_mfma_f32_32x32x16_bf16 v[240:255], v[208:211], v[212:215], v[240:255]\n"
        "ds_read_b64_tr_b16 v[176:177], %9 offset:32768\n"
        "ds_read_b64_tr_b16 v[178:179], %9 offset:33792\n"
        "ds_read_b128 v[180:183], %10 offset:16384\n"
        "v_mfma_f32_32x32x16_bf16 v[240:255], v[216:219], v[220:223], v[240:255]\n"
        "ds_read_b64_tr_b16 v[184:185], %9 offset:36864\n"
        "ds_read_b64_tr_b16 v[186:187], %9 offset:37888\n"
        "ds_read_b128 v[188:191], %11 offset:16384\n"
        "v_mfma_f32_32x32x16_bf16 v[240:255], v[224:227], v[228:231], v[240:255]\n"
        "ds_read_b64_tr_b16 v[192:193], %9 offset:40960\n"
        "ds_read_b64_tr_b16 v[194:195], %9 offset:41984\n"
        "ds_read_b128 v[196:199], %12 offset:16384\n"
        "v_mfma_f32_32x32x16_bf16 v[240:255], v[232:235], v[236:239], v[240:255]\n"
        "ds_read_b64_tr_b16 v[200:201], %9 offset:45056\n"
        "ds_read_b64_tr_b16 v[202:203], %9 offset:46080\n"
        "ds_read_b128 v[204:207], %13 offset:16384\n"
        "s_cmp_lt_u32 %17, 2\n"
        "s_cbranch_scc1 13f\n"
        "s_waitcnt vmcnt(18) lgkmcnt(0)\n"
        "s_barrier\n"
        "s_mov_b32 m0, s45\n"
        "s_add_u32 s45, s45, 1024\n"
        "global_load_lds_dwordx4 %14, s[42:43]\n"
        "s_add_u32 s42, s42, 4096\n"
        "s_addc_u32 s43, s43, 0\n"
        "s_mov_b32 m0, s45\n"
        "s_add_u32 s45, s45, 1024\n"
        "global_load_lds_dwordx4 %14, s[42:43]\n"
        "s_add_u32 s42, s42, 4096\n"
        "s_addc_u32 s43, s43, 0\n"
        "s_mov_b32 m0, s45\n"
        "s_add_u32 s45, s45, 1024\n"
        "global_load_lds_dwordx4 %14, s[42:43]\n"
        "s_add_u32 s42, s42, 4096\n"
        "s_addc_u32 s43, s43, 0\n"
        "s_mov_b32 m0, s45\n"
        "s_add_u32 s45, s45, 1024\n"
        "global_load_lds_dwordx4 %14, s[42:43]\n"
        "s_add_u32 s42, s42, 4096\n"
        "s_addc_u32 s43, s43, 0\n"
        "s_add_u32 s42, s42, 49152\n"
        "s_addc_u32 s43, s43, 0\n"
        "s_add_u32 s45, s45, 12288\n"
        "s_branch 23f\n"
        "13:\n"
        "s_waitcnt lgkmcnt(0)\n"
        "s_barrier\n"
        "23:\n"
        "v_mfma_f32_32x32x16_bf16 v[240:255], v[176:179], v[180:183], v[240:255]\n"
        "ds_read_b64_tr_b16 v[208:209], %9 offset:49152\n"
        "ds_read_b64_tr_b16 v[210:211], %9 offset:50176\n"
        "ds_read_b128 v[212:215], %10 offset:24576\n"
        "v_mfma_f32_32x32x16_bf16 v[240:255], v[184:187], v[188:191], v[240:255]\n"
        "ds_read_b64_tr_b16 v[216:217], %9 offset:53248\n"
        "ds_read_b64_tr_b16 v[218:219], %9 offset:54272\n"
        "ds_read_b128 v[220:223], %11 offset:24576\n"
        "v_mfma_f32_32x32x16_bf16 v[240:255], v[192:195], v[196:199], v[240:255]\n"
        "ds_read_b64_tr_b16 v[224:225], %9 offset:57344\n"
        "ds_read_b64_tr_b16 v[226:227], %9 offset:58368\n"
        "ds_read_b128 v[228:231], %12 offset:24576\n"
        "v_mfma_f32_32x32x16_bf16 v[240:255], v[200:203], v[204:207], v[240:255]\n"
        "ds_read_b64_tr_b16 v[232:233], %9 offset:61440\n"
        "ds_read_b64_tr_b16 v[234:235], %9 offset:62464\n"
        "ds_read_b128 v[236:239], %13 offset:24576\n"
        "s_cmp_lt_u32 %17, 2\n"
        "s_cbranch_scc1 14f\n"
        "s_waitcnt vmcnt(15) lgkmcnt(0)\n"
        "s_barrier\n"
        "s_branch 24f\n"
        "14:\n"
        "s_waitcnt lgkmcnt(0)\n"
        "s_barrier\n"
        "24:\n"
        "v_add_u32 %9, 0x10000, %9\n"
        "v_mfma_f32_32x32x16_bf16 v[240:255], v[208:211], v[212:215], v[240:255]\n"
        "ds_read_b64_tr_b16 v[176:177], %9 offset:0\n"
        "ds_read_b64_tr_b16 v[178:179], %9 offset:1024\n"
        "ds_read_b128 v[180:183], %10 offset:32768\n"
        "v_mfma_f32_32x32x16_bf16 v[240:255], v[216:219], v[220:223], v[240:255]\n"
        "ds_read_b64_tr_b16 v[184:185], %9 offset:4096\n"
        "ds_read_b64_tr_b16 v[186:187], %9 offset:5120\n"
        "ds_read_b128 v[188:191], %11 offset:32768\n"
        "v_mfma_f32_32x32x16_bf16 v[240:255], v[224:227], v[228:231], v[240:255]\n"
        "ds_read_b64_tr_b16 v[192:193], %9 offset:8192\n"
        "ds_read_b64_tr_b16 v[194:195], %9 offset:9216\n"
        "ds_read_b128 v[196:199], %12 offset:32768\n"
        "v_mfma_f32_32x32x16_bf16 v[240:255], v[232:235], v[236:239], v[240:255]\n"
        "ds_read_b64_tr_b16 v[200:201], %9 offset:12288\n"
        "ds_read_b64_tr_b16 v[202:203], %9 offset:13312\n"
        "ds_read_b128 v[204:207], %13 offset:32768\n"
        "s_cmp_lt_u32 %17, 2\n"
        "s_cbranch_scc1 15f\n"
        "s_waitcnt vmcnt(11) lgkmcnt(0)\n"
        "s_barrier\n"
        "s_branch 25f\n"
        "15:\n"
        "s_waitcnt lgkmcnt(0)\n"
        "s_barrier\n"
        "25:\n"
        "v_mfma_f32_32x32x16_bf16 v[240:255], v[176:179], v[180:183], v[240:255]\n"
        "ds_read_b64_tr_b16 v[208:209], %9 offset:16384\n"
        "ds_read_b64_tr_b16 v[210:211], %9 offset:17408\n"
        "ds_read_b128 v[212:215], %10 offset:40960\n"
        "v_mfma_f32_32x32x16_bf16 v[240:255], v[184:187], v[188:191], v[240:255]\n"
        "ds_read_b64_tr_b16 v[216:217], %9 offset:20480\n"
        "ds_read_b64_tr_b16 v[218:219], %9 offset:21504\n"
        "ds_read_b128 v[220:223], %11 offset:40960\n"
        "v_mfma_f32_32x32x16_bf16 v[240:255], v[192:195], v[196:199], v[240:255]\n"
        "ds_read_b64_tr_b16 v[224:225], %9 offset:24576\n"
        "ds_read_b64_tr_b16 v[226:227], %9 offset:25600\n"
        "ds_read_b128 v[228:231], %12 offset:40960\n"
        "v_mfma_f32_32x32x16_bf16 v[240:255], v[200:203], v[204:207], v[240:255]\n"
        "ds_read_b64_tr_b16 v[232:233], %9 offset:28672\n"
        "ds_read_b64_tr_b16 v[234:235], %9 offset:29696\n"
        "ds_read_b128 v[236:239], %13 offset:40960\n"
        "s_cmp_lt_u32 %17, 2\n"
        "s_cbranch_scc1 16f\n"
        "s_waitcnt vmcnt(4) lgkmcnt(0)\n"
        "s_barrier\n"
        "s_branch 26f\n"
        "16:\n"
        "s_waitcnt lgkmcnt(0)\n"
        "s_barrier\n"
        "26:\n"
        "v_subrev_u32 %9, 0x10000, %9\n"
        "v_mfma_f32_32x32x16_bf16 v[240:255], v[208:211], v[212:215], v[240:255]\n"
        "ds_read_b64_tr_b16 v[176:177], %9 offset:0\n"
        "ds_read_b64_tr_b16 v[178:179], %9 offset:1024\n"
        "ds_read_b128 v[180:183], %10 offset:49152\n"
        "v_mfma_f32_32x32x16_bf16 v[240:255], v[216:219], v[220:223], v[240:255]\n"
        "ds_read_b64_tr_b16 v[184:185], %9 offset:4096\n"
        "ds_read_b64_tr_b16 v[186:187], %9 offset:5120\n"
        "ds_read_b128 v[188:191], %11 offset:49152\n"
        "v_mfma_f32_32x32x16_bf16 v[240:255], v[224:227], v[228:231], v[240:255]\n"
        "ds_read_b64_tr_b16 v[192:193], %9 offset:8192\n"
        "ds_read_b64_tr_b16 v[194:195], %9 offset:9216\n"
        "ds_read_b128 v[196:199], %12 offset:49152\n"
        "v_mfma_f32_32x32x16_bf16 v[240:255], v[232:235], v[236:239], v[240:255]\n"
        "ds_read_b64_tr_b16 v[200:201], %9 offset:12288\n"
        "ds_read_b64_tr_b16 v[202:203], %9 offset:13312\n"
        "ds_read_b128 v[204:207], %13 offset:49152\n"
        "s_cmp_lg_u32 %17, 0\n"
        "s_cbranch_scc1 31f\n"
        "global_load_dwordx2 %8, %21, %19 sc1\n"
        "31:\n"
        "s_cmp_lt_u32 %17, 2\n"
        "s_cbranch_scc1 17f\n"
        "s_waitcnt vmcnt(0) lgkmcnt(0)\n"
        "s_barrier\n"
        "s_branch 27f\n"
        "17:\n"
        "s_waitcnt lgkmcnt(0)\n"
        "s_barrier\n"
        "27:\n"
        "v_mfma_f32_32x32x16_bf16 v[240:255], v[176:179], v[180:183], v[240:255]\n"
        "ds_read_b64_tr_b16 v[208:209], %9 offset:16384\n"
        "ds_read_b64_tr_b16 v[210:211], %9 offset:17408\n"
        "ds_read_b128 v[212:215], %10 offset:57344\n"
        "v_mfma_f32_32x32x16_bf16 v[240:255], v[184:187], v[188:191], v[240:255]\n"
        "ds_read_b64_tr_b16 v[216:217], %9 offset:20480\n"
        "ds_read_b64_tr_b16 v[218:219], %9 offset:21504\n"
        "ds_read_b128 v[220:223], %11 offset:57344\n"
        "v_mfma_f32_32x32x16_bf16 v[240:255], v[192:195], v[196:199], v[240:255]\n"
        "ds_read_b64_tr_b16 v[224:225], %9 offset:24576\n"
        "ds_read_b64_tr_b16 v[226:227], %9 offset:25600\n"
        "ds_read_b128 v[228:231], %12 offset:57344\n"
        "v_mfma_f32_32x32x16_bf16 v[240:255], v[200:203], v[204:207], v[240:255]\n"
        "ds_read_b64_tr_b16 v[232:233], %9 offset:28672\n"
        "ds_read_b64_tr_b16 v[234:235], %9 offset:29696\n"
        "ds_read_b128 v[236:239], %13 offset:57344\n"
        "s_waitcnt lgkmcnt(0)\n"
        "v_mfma_f32_32x32x16_bf16 v[240:255], v[208:211], v[212:215], v[240:255]\n"
        "v_mfma_f32_32x32x16_bf16 v[240:255], v[216:219], v[220:223], v[240:255]\n"
        "v_mfma_f32_32x32x16_bf16 v[240:255], v[224:227], v[228:231], v[240:255]\n"
        "v_mfma_f32_32x32x16_bf16 v[240:255], v[232:235], v[236:239], v[240:255]\n"
        "s_mov_b32 m0, s44\n"
        "s_waitcnt vmcnt(0)\n"
        "s_nop 15\n"
        "v_mul_f32 v240, 0x3eb8aa3b, v240\n"
        "v_mul_f32 v241, 0x3eb8aa3b, v241\n"
        "v_mul_f32 v242, 0x3eb8aa3b, v242\n"
        "v_mul_f32 v243, 0x3eb8aa3b, v243\n"
        "v_mul_f32 v244, 0x3eb8aa3b, v244\n"
        "v_mul_f32 v245, 0x3eb8aa3b, v245\n"
        "v_mul_f32 v246, 0x3eb8aa3b, v246\n"
        "v_mul_f32 v247, 0x3eb8aa3b, v247\n"
        "v_mul_f32 v248, 0x3eb8aa3b, v248\n"
        "v_mul_f32 v249, 0x3eb8aa3b, v249\n"
        "v_mul_f32 v250, 0x3eb8aa3b, v250\n"
        "v_mul_f32 v251, 0x3eb8aa3b, v251\n"
        "v_mul_f32 v252, 0x3eb8aa3b, v252\n"
        "v_mul_f32 v253, 0x3eb8aa3b, v253\n"
        "v_mul_f32 v254, 0x3eb8aa3b, v254\n"
        "v_mul_f32 v255, 0x3eb8aa3b, v255\n"
        "v_cvt_pk_bf16_f32 %0, v240, v241\n"
        "v_cvt_pk_bf16_f32 %1, v242, v243\n"
        "v_cvt_pk_bf16_f32 %2, v244, v245\n"
        "v_cvt_pk_bf16_f32 %3, v246, v247\n"
        "v_cvt_pk_bf16_f32 %4, v248, v249\n"
        "v_cvt_pk_bf16_f32 %5, v250, v251\n"
        "v_cvt_pk_bf16_f32 %6, v252, v253\n"
        "v_cvt_pk_bf16_f32 %7, v254, v255\n"
        : "=&v"(qa[0]), "=&v"(qa[1]), "=&v"(qa[2]), "=&v"(qa[3]), "=&v"(qb[0]), "=&v"(qb[1]), "=&v"(qb[2]), "=&v"(qb[3]), "=&v"(flags), "+v"(wb)
        : "v"(xb), "v"(xb ^ 32), "v"(xb ^ 64), "v"(xb ^ 96), "v"(voff), "s"(wq0), "s"(ring), "s"(role), "v"(epoch), "s"(ws + WS_KVF), "v"(d.b * KVF_BRANCH + kvf_off(d.rb * 4 + d.hg)), "v"(d.b * KVF_BRANCH + kvf_off(2 * l)), "s"(x + (size_t)d.rb * 65536), "s"(lds + L_X), "v"(x_lane_off(l, q & 1)), "v"(x_lane_off(l, (q + 1) & 1)), "s"(xp[0]), "s"(xp[1]), "s"(xp[2]), "s"((blockIdx.x >> 3) >= 16 ? wq + (size_t)((((blockIdx.x >> 3) & 15) * 4 + q) * 2) * 4096 : (const char*)nullptr)
        : "v176", "v177", "v178", "v179", "v180", "v181", "v182", "v183", "v184", "v185", "v186", "v187", "v188", "v189", "v190", "v191", "v192", "v193", "v194", "v195", "v196", "v197", "v198", "v199", "v200", "v201", "v202", "v203", "v204", "v205", "v206", "v207", "v208", "v209", "v210", "v211", "v212", "v213", "v214", "v215", "v216", "v217", "v218", "v219", "v220", "v221", "v222", "v223", "v224", "v225", "v226", "v227", "v228", "v229", "v230", "v231", "v232", "v233", "v234", "v235", "v236", "v237", "v238", "v239", "v240", "v241", "v242", "v243", "v244", "v245", "v246", "v247", "v248", "v249", "v250", "v251", "v252", "v253", "v254", "v255", "s42", "s43", "s44", "s45", "s46", "s47", "s48", "s49", "s50", "s51", "s52", "s53", "s54", "s55", "s56", "scc", "memory");
    asm volatile("" : "+v"(epoch));
}

__device__ __forceinline__ unsigned wo_seg(unsigned lds, unsigned s) {
    return lds + (s < 7 ? s * 8192 : s < 14 ? L_V + (s - 7) * 8192 : L_SCR + (s - 14) * 8192);
}
__device__ __forceinline__ unsigned wo_lane_off(int l) {
    const unsigned row = l >> 3, sc = l & 7;
    return row * 1024 + (sc ^ (((row >> 1) & 1) << 2)) * 16;
}
__device__ __forceinline__ void dma_kv_first(const Ids& d, unsigned lds, const char* kv) {
    const unsigned img = (d.w >> 2) * 65536, q = d.w & 3, lo = d.lane * 16;
    glds16_sc1(kv + img + q * 1024, lo, lds + img + q * 1024);
    glds16_sc1(kv + img + (q + 4) * 1024, lo, lds + img + (q + 4) * 1024);
    if (q == 0) glds16_sc1(kv + img + 8 * 1024, lo, lds + img + 8 * 1024);
}
__device__ __forceinline__ unsigned kv_rest_off(const Ids& d) {
    const unsigned q = d.w & 3;
    return (d.w >> 2) * 65536 + (q == 3 ? 50 : 9 + 14 * q) * 1024;
}

__device__ __forceinline__ void init_const_rows(unsigned lds) {
    if (threadIdx.x < 128) {
        const unsigned blk = threadIdx.x >> 2, pos = threadIdx.x & 3;
        const unsigned v = (pos & 1) ? 0u : 0x3F803F80u;
        const u32x4 vv = {v, v, v, v};
        *(LAS u32x4*)(size_t)(lds + L_ONE + blk * 512 + (pos >> 1) * 64 + (pos & 1) * 16) = vv;
    }
}

__device__ __forceinline__ void store_o(const char* dst, int l, const f32x16& oa, const f32x16& ob) {
    const float ia = __builtin_amdgcn_rcpf(oa[8]), ib = __builtin_amdgcn_rcpf(ob[8]);
    const u32x4 fa = {pack2(oa[0] * ia, oa[1] * ia), pack2(oa[2] * ia, oa[3] * ia), pack2(oa[4] * ia, oa[5] * ia), pack2(oa[6] * ia, oa[7] * ia)};
    const u32x4 fb = {pack2(ob[0] * ib, ob[1] * ib), pack2(ob[2] * ib, ob[3] * ib), pack2(ob[4] * ib, ob[5] * ib), pack2(ob[6] * ib, ob[7] * ib)};
    store16_sc1(dst, l * 16, fa);
    store16_sc1(dst + 1024, l * 16, fb);
}

__device__ __forceinline__ void attn_pass(unsigned lds, int lane, u32x4 qa, u32x4 qb, float ma, float mb, const char* kv_rest, unsigned lds_rest,
                                          const char* wo, unsigned seg0, unsigned seg1, f32x16& oa, f32x16& ob) {
    const int rr = lane & 31, h = lane >> 5;
    unsigned aK = lds + L_K + lane * 16;
    const unsigned cst = (rr == 16) ? 0u : (rr == 20) ? 64u : (rr > 20 && rr < 28) ? 80u : 16u;
    unsigned aV = rr < 16 ? lds + L_V + h * 256 + rr * 16 : lds + L_ONE + cst;
    const unsigned incV = rr < 16 ? 8u * 1024u : 0u;
    f32x16 ca, cb;
#pragma unroll
    for (int i = 0; i < 16; ++i) { ca[i] = -ma; cb[i] = -mb; oa[i] = 0.f; ob[i] = 0.f; }
    asm volatile(
        "v_mov_b32 v252, 0xdddddddd\n"
        "v_mov_b32 v224, 0\n"
        "v_mov_b32 v225, 0\n"
        "v_mov_b32 v226, 0\n"
        "v_mov_b32 v227, 0\n"
        "v_mov_b32 v228, 0\n"
        "v_mov_b32 v229, 0\n"
        "v_mov_b32 v230, 0\n"
        "v_mov_b32 v231, 0\n"
        "v_mov_b32 v232, 0\n"
        "v_mov_b32 v233, 0\n"
        "v_mov_b32 v234, 0\n"
        "v_mov_b32 v235, 0\n"
        "v_mov_b32 v236, 0\n"
        "v_mov_b32 v237, 0\n"
        "v_mov_b32 v238, 0\n"
        "v_mov_b32 v239, 0\n"
        "s_mov_b32 s41, m0\n"
        "s_mov_b64 s[42:43], %13\n"
        "s_mov_b32 s44, %14\n"
        "s_waitcnt vmcnt(0)\n"
        "s_barrier\n"
        "ds_read_b128 v[240:243], %2\n"
        "ds_read_b128 v[244:247], %3\n"
        "ds_read_b128 v[248:251], %3 offset:512\n"
        "s_waitcnt lgkmcnt(0)\n"
        "v_mfma_f32_32x32x16_bf16 v[208:223], v[240:243], %4, %6\n"
        "s_mov_b32 s40, 6\n"
        "s_nop 15\n"
        "v_exp_f32 v208, v208\n"
        "v_exp_f32 v209, v209\n"
        "v_smfmac_f32_32x32x32_bf16 %1, v[248:251], v[232:239], v252\n"
        "v_exp_f32 v210, v210\n"
        "ds_read_b128 v[248:251], %3 offset:512\n"
        "v_exp_f32 v211, v211\n"
        "v_exp_f32 v212, v212\n"
        "v_exp_f32 v213, v213\n"
        "v_mfma_f32_32x32x16_bf16 v[224:239], v[240:243], %5, %7\n"
        "v_exp_f32 v214, v214\n"
        "ds_read_b128 v[240:243], %2 offset:1024\n"
        "v_exp_f32 v215, v215\n"
        "v_exp_f32 v216, v216\n"
        "v_exp_f32 v217, v217\n"
        "s_waitcnt lgkmcnt(2)\n"
        "v_smfmac_f32_32x32x32_bf16 %0, v[244:247], v[208:215], v252\n"
        "v_exp_f32 v218, v218\n"
        "v_exp_f32 v219, v219\n"
        "s_mov_b32 m0, s44\n"
        "s_add_u32 s44, s44, 1024\n"
        "global_load_lds_dwordx4 %15, s[42:43] sc1\n"
        "s_add_u32 s42, s42, 1024\n"
        "s_addc_u32 s43, s43, 0\n"
        "v_exp_f32 v220, v220\n"
        "v_exp_f32 v221, v221\n"
        "v_exp_f32 v222, v222\n"
        "s_mov_b32 m0, s44\n"
        "s_add_u32 s44, s44, 1024\n"
        "global_load_lds_dwordx4 %15, s[42:43] sc1\n"
        "s_add_u32 s42, s42, 1024\n"
        "s_addc_u32 s43, s43, 0\n"
        "v_exp_f32 v223, v223\n"
        "v_exp_f32 v224, v224\n"
        "v_exp_f32 v225, v225\n"
        "s_waitcnt lgkmcnt(1)\n"
        "v_smfmac_f32_32x32x32_bf16 %0, v[248:251], v[216:223], v252\n"
        "v_exp_f32 v226, v226\n"
        "v_exp_f32 v227, v227\n"
        "v_exp_f32 v228, v228\n"
        "v_exp_f32 v229, v229\n"
        "s_waitcnt lgkmcnt(0)\n"
        "v_mfma_f32_32x32x16_bf16 v[208:223], v[240:243], %4, %6\n"
        "v_exp_f32 v230, v230\n"
        "v_exp_f32 v231, v231\n"
        "v_exp_f32 v232, v232\n"
        "v_exp_f32 v233, v233\n"
        "v_smfmac_f32_32x32x32_bf16 %1, v[244:247], v[224:231], v252\n"
        "v_exp_f32 v234, v234\n"
        "ds_read_b128 v[244:247], %3 offset:1024\n"
        "v_exp_f32 v235, v235\n"
        "s_mov_b32 m0, s44\n"
        "s_add_u32 s44, s44, 1024\n"
        "global_load_lds_dwordx4 %15, s[42:43] sc1\n"
        "s_add_u32 s42, s42, 1024\n"
        "s_addc_u32 s43, s43, 0\n"
        "v_exp_f32 v236, v236\n"
        "v_exp_f32 v237, v237\n"
        "v_exp_f32 v238, v238\n"
        "s_mov_b32 m0, s44\n"
        "s_add_u32 s44, s44, 1024\n"
        "global_load_lds_dwordx4 %15, s[42:43] sc1\n"
        "s_add_u32 s42, s42, 1024\n"
        "s_addc_u32 s43, s43, 0\n"
        "v_exp_f32 v239, v239\n"
        "v_exp_f32 v208, v208\n"
        "v_exp_f32 v209, v209\n"
        "v_smfmac_f32_32x32x32_bf16 %1, v[248:251], v[232:239], v252\n"
        "v_exp_f32 v210, v210\n"
        "ds_read_b128 v[248:251], %3 offset:1536\n"
        "v_exp_f32 v211, v211\n"
        "v_exp_f32 v212, v212\n"
        "v_exp_f32 v213, v213\n"
        "v_mfma_f32_32x32x16_bf16 v[224:239], v[240:243], %5, %7\n"
        "v_exp_f32 v214, v214\n"
        "ds_read_b128 v[240:243], %2 offset:2048\n"
        "v_exp_f32 v215, v215\n"
        "v_exp_f32 v216, v216\n"
        "v_exp_f32 v217, v217\n"
        "s_waitcnt lgkmcnt(2)\n"
        "v_smfmac_f32_32x32x32_bf16 %0, v[244:247], v[208:215], v252\n"
        "v_exp_f32 v218, v218\n"
        "v_exp_f32 v219, v219\n"
        "s_mov_b32 m0, s44\n"
        "s_add_u32 s44, s44, 1024\n"
        "global_load_lds_dwordx4 %15, s[42:43] sc1\n"
        "s_add_u32 s42, s42, 1024\n"
        "s_addc_u32 s43, s43, 0\n"
        "v_exp_f32 v220, v220\n"
        "v_exp_f32 v221, v221\n"
        "v_exp_f32 v222, v222\n"
        "s_mov_b32 m0, s44\n"
        "s_add_u32 s44, s44, 1024\n"
        "global_load_lds_dwordx4 %15, s[42:43] sc1\n"
        "s_add_u32 s42, s42, 1024\n"
        "s_addc_u32 s43, s43, 0\n"
        "v_exp_f32 v223, v223\n"
        "v_exp_f32 v224, v224\n"
        "v_exp_f32 v225, v225\n"
        "s_waitcnt lgkmcnt(1)\n"
        "v_smfmac_f32_32x32x32_bf16 %0, v[248:251], v[216:223], v252\n"
        "v_exp_f32 v226, v226\n"
        "v_exp_f32 v227, v227\n"
        "v_exp_f32 v228, v228\n"
        "v_exp_f32 v229, v229\n"
        "s_waitcnt lgkmcnt(0)\n"
        "v_mfma_f32_32x32x16_bf16 v[208:223], v[240:243], %4, %6\n"
        "v_exp_f32 v230, v230\n"
        "v_exp_f32 v231, v231\n"
        "v_exp_f32 v232, v232\n"
        "v_exp_f32 v233, v233\n"
        "v_smfmac_f32_32x32x32_bf16 %1, v[244:247], v[224:231], v252\n"
        "v_exp_f32 v234, v234\n"
        "ds_read_b128 v[244:247], %3 offset:2048\n"
        "v_exp_f32 v235, v235\n"
        "s_mov_b32 m0, s44\n"
        "s_add_u32 s44, s44, 1024\n"
        "global_load_lds_dwordx4 %15, s[42:43] sc1\n"
        "s_add_u32 s42, s42, 1024\n"
        "s_addc_u32 s43, s43, 0\n"
        "v_exp_f32 v236, v236\n"
        "v_exp_f32 v237, v237\n"
        "v_exp_f32 v238, v238\n"
        "s_mov_b32 m0, s44\n"
        "s_add_u32 s44, s44, 1024\n"
        "global_load_lds_dwordx4 %15, s[42:43] sc1\n"
        "s_add_u32 s42, s42, 1024\n"
        "s_addc_u32 s43, s43, 0\n"
        "v_exp_f32 v239, v239\n"
        "v_exp_f32 v208, v208\n"
        "v_exp_f32 v209, v209\n"
        "v_smfmac_f32_32x32x32_bf16 %1, v[248:251], v[232:239], v252\n"
        "v_exp_f32 v210, v210\n"
        "ds_read_b128 v[248:251], %3 offset:2560\n"
        "v_exp_f32 v211, v211\n"
        "v_exp_f32 v212, v212\n"
        "v_exp_f32 v213, v213\n"
        "v_mfma_f32_32x32x16_bf16 v[224:239], v[240:243], %5, %7\n"
        "v_exp_f32 v214, v214\n"
        "ds_read_b128 v[240:243], %2 offset:3072\n"
        "v_exp_f32 v215, v215\n"
        "v_exp_f32 v216, v216\n"
        "v_exp_f32 v217, v217\n"
        "s_waitcnt lgkmcnt(2)\n"
        "v_smfmac_f32_32x32x32_bf16 %0, v[244:247], v[208:215], v252\n"
        "v_exp_f32 v218, v218\n"
        "v_exp_f32 v219, v219\n"
        "s_mov_b32 m0, s44\n"
        "s_add_u32 s44, s44, 1024\n"
        "global_load_lds_dwordx4 %15, s[42:43] sc1\n"
        "s_add_u32 s42, s42, 1024\n"
        "s_addc_u32 s43, s43, 0\n"
        "v_exp_f32 v220, v220\n"
        "v_exp_f32 v221, v221\n"
        "v_exp_f32 v222, v222\n"
        "s_mov_b32 m0, s44\n"
        "s_add_u32 s44, s44, 1024\n"
        "global_load_lds_dwordx4 %15, s[42:43] sc1\n"
        "s_add_u32 s42, s42, 1024\n"
        "s_addc_u32 s43, s43, 0\n"
        "v_exp_f32 v223, v223\n"
        "v_exp_f32 v224, v224\n"
        "v_exp_f32 v225, v225\n"
        "s_waitcnt lgkmcnt(1)\n"
        "v_smfmac_f32_32x32x32_bf16 %0, v[248:251], v[216:223], v252\n"
        "v_exp_f32 v226, v226\n"
        "v_exp_f32 v227, v227\n"
        "v_exp_f32 v228, v228\n"
        "v_exp_f32 v229, v229\n"
        "s_waitcnt lgkmcnt(0)\n"
        "v_mfma_f32_32x32x16_bf16 v[208:223], v[240:243], %4, %6\n"
        "v_exp_f32 v230, v230\n"
        "v_exp_f32 v231, v231\n"
        "v_exp_f32 v232, v232\n"
        "v_exp_f32 v233, v233\n"
        "v_smfmac_f32_32x32x32_bf16 %1, v[244:247], v[224:231], v252\n"
        "v_exp_f32 v234, v234\n"
        "ds_read_b128 v[244:247], %3 offset:3072\n"
        "v_exp_f32 v235, v235\n"
        "s_mov_b32 m0, s44\n"
        "s_add_u32 s44, s44, 1024\n"
        "global_load_lds_dwordx4 %15, s[42:43] sc1\n"
        "s_add_u32 s42, s42, 1024\n"
        "s_addc_u32 s43, s43, 0\n"
        "v_exp_f32 v236, v236\n"
        "v_exp_f32 v237, v237\n"
        "v_exp_f32 v238, v238\n"
        "s_mov_b32 m0, s44\n"
        "s_add_u32 s44, s44, 1024\n"
        "global_load_lds_dwordx4 %15, s[42:43] sc1\n"
        "s_add_u32 s42, s42, 1024\n"
        "s_addc_u32 s43, s43, 0\n"
        "v_exp_f32 v239, v239\n"
        "v_exp_f32 v208, v208\n"
        "v_exp_f32 v209, v209\n"
        "v_smfmac_f32_32x32x32_bf16 %1, v[248:251], v[232:239], v252\n"
        "v_exp_f32 v210, v210\n"
        "ds_read_b128 v[248:251], %3 offset:3584\n"
        "v_exp_f32 v211, v211\n"
        "v_exp_f32 v212, v212\n"
        "v_exp_f32 v213, v213\n"
        "v_mfma_f32_32x32x16_bf16 v[224:239], v[240:243], %5, %7\n"
        "v_exp_f32 v214, v214\n"
        "ds_read_b128 v[240:243], %2 offset:4096\n"
        "v_exp_f32 v215, v215\n"
        "v_exp_f32 v216, v216\n"
        "v_exp_f32 v217, v217\n"
        "s_waitcnt lgkmcnt(2)\n"
        "v_smfmac_f32_32x32x32_bf16 %0, v[244:247], v[208:215], v252\n"
        "v_exp_f32 v218, v218\n"
        "v_exp_f32 v219, v219\n"
        "s_mov_b32 m0, s44\n"
        "s_add_u32 s44, s44, 1024\n"
        "global_load_lds_dwordx4 %15, s[42:43] sc1\n"
        "s_add_u32 s42, s42, 1024\n"
        "s_addc_u32 s43, s43, 0\n"
        "v_exp_f32 v220, v220\n"
        "v_exp_f32 v221, v221\n"
        "v_exp_f32 v222, v222\n"
        "s_mov_b32 m0, s44\n"
        "s_add_u32 s44, s44, 1024\n"
        "global_load_lds_dwordx4 %15, s[42:43] sc1\n"
        "s_add_u32 s42, s42, 1024\n"
        "s_addc_u32 s43, s43, 0\n"
        "v_exp_f32 v223, v223\n"
        "v_exp_f32 v224, v224\n"
        "v_exp_f32 v225, v225\n"
        "s_waitcnt lgkmcnt(1)\n"
        "v_smfmac_f32_32x32x32_bf16 %0, v[248:251], v[216:223], v252\n"
        "v_exp_f32 v226, v226\n"
        "v_exp_f32 v227, v227\n"
        "v_exp_f32 v228, v228\n"
        "v_exp_f32 v229, v229\n"
        "s_waitcnt lgkmcnt(0)\n"
        "v_mfma_f32_32x32x16_bf16 v[208:223], v[240:243], %4, %6\n"
        "v_exp_f32 v230, v230\n"
        "v_exp_f32 v231, v231\n"
        "v_exp_f32 v232, v232\n"
        "v_exp_f32 v233, v233\n"
        "v_smfmac_f32_32x32x32_bf16 %1, v[244:247], v[224:231], v252\n"
        "v_exp_f32 v234, v234\n"
        "ds_read_b128 v[244:247], %3 offset:4096\n"
        "v_exp_f32 v235, v235\n"
        "v_exp_f32 v236, v236\n"
        "v_exp_f32 v237, v237\n"
        "v_exp_f32 v238, v238\n"
        "v_exp_f32 v239, v239\n"
        "v_exp_f32 v208, v208\n"
        "v_exp_f32 v209, v209\n"
        "v_smfmac_f32_32x32x32_bf16 %1, v[248:251], v[232:239], v252\n"
        "v_exp_f32 v210, v210\n"
        "ds_read_b128 v[248:251], %3 offset:4608\n"
        "v_exp_f32 v211, v211\n"
        "v_exp_f32 v212, v212\n"
        "v_exp_f32 v213, v213\n"
        "v_mfma_f32_32x32x16_bf16 v[224:239], v[240:243], %5, %7\n"
        "v_exp_f32 v214, v214\n"
        "ds_read_b128 v[240:243], %2 offset:5120\n"
        "v_exp_f32 v215, v215\n"
        "v_exp_f32 v216, v216\n"
        "v_exp_f32 v217, v217\n"
        "s_waitcnt lgkmcnt(2)\n"
        "v_smfmac_f32_32x32x32_bf16 %0, v[244:247], v[208:215], v252\n"
        "v_exp_f32 v218, v218\n"
        "v_exp_f32 v219, v219\n"
        "v_exp_f32 v220, v220\n"
        "v_exp_f32 v221, v221\n"
        "v_exp_f32 v222, v222\n"
        "v_exp_f32 v223, v223\n"
        "v_exp_f32 v224, v224\n"
        "v_exp_f32 v225, v225\n"
        "s_waitcnt lgkmcnt(1)\n"
        "v_smfmac_f32_32x32x32_bf16 %0, v[248:251], v[216:223], v252\n"
        "v_exp_f32 v226, v226\n"
        "v_exp_f32 v227, v227\n"
        "v_exp_f32 v228, v228\n"
        "v_exp_f32 v229, v229\n"
        "s_waitcnt lgkmcnt(0)\n"
        "v_mfma_f32_32x32x16_bf16 v[208:223], v[240:243], %4, %6\n"
        "v_exp_f32 v230, v230\n"
        "v_exp_f32 v231, v231\n"
        "v_exp_f32 v232, v232\n"
        "v_exp_f32 v233, v233\n"
        "v_smfmac_f32_32x32x32_bf16 %1, v[244:247], v[224:231], v252\n"
        "v_exp_f32 v234, v234\n"
        "ds_read_b128 v[244:247], %3 offset:5120\n"
        "v_exp_f32 v235, v235\n"
        "v_exp_f32 v236, v236\n"
        "v_exp_f32 v237, v237\n"
        "v_exp_f32 v238, v238\n"
        "v_exp_f32 v239, v239\n"
        "v_exp_f32 v208, v208\n"
        "v_exp_f32 v209, v209\n"
        "v_smfmac_f32_32x32x32_bf16 %1, v[248:251], v[232:239], v252\n"
        "v_exp_f32 v210, v210\n"
        "ds_read_b128 v[248:251], %3 offset:5632\n"
        "v_exp_f32 v211, v211\n"
        "v_exp_f32 v212, v212\n"
        "v_exp_f32 v213, v213\n"
        "v_mfma_f32_32x32x16_bf16 v[224:239], v[240:243], %5, %7\n"
        "v_exp_f32 v214, v214\n"
        "ds_read_b128 v[240:243], %2 offset:6144\n"
        "v_exp_f32 v215, v215\n"
        "v_exp_f32 v216, v216\n"
        "v_exp_f32 v217, v217\n"
        "s_waitcnt lgkmcnt(2)\n"
        "v_smfmac_f32_32x32x32_bf16 %0, v[244:247], v[208:215], v252\n"
        "v_exp_f32 v218, v218\n"
        "v_exp_f32 v219, v219\n"
        "v_exp_f32 v220, v220\n"
        "v_exp_f32 v221, v221\n"
        "v_exp_f32 v222, v222\n"
        "v_exp_f32 v223, v223\n"
        "v_exp_f32 v224, v224\n"
        "v_exp_f32 v225, v225\n"
        "s_waitcnt lgkmcnt(1)\n"
        "v_smfmac_f32_32x32x32_bf16 %0, v[248:251], v[216:223], v252\n"
        "v_exp_f32 v226, v226\n"
        "v_exp_f32 v227, v227\n"
        "v_exp_f32 v228, v228\n"
        "v_exp_f32 v229, v229\n"
        "s_waitcnt lgkmcnt(0)\n"
        "v_mfma_f32_32x32x16_bf16 v[208:223], v[240:243], %4, %6\n"
        "v_exp_f32 v230, v230\n"
        "v_exp_f32 v231, v231\n"
        "v_exp_f32 v232, v232\n"
        "v_exp_f32 v233, v233\n"
        "v_smfmac_f32_32x32x32_bf16 %1, v[244:247], v[224:231], v252\n"
        "v_exp_f32 v234, v234\n"
        "ds_read_b128 v[244:247], %3 offset:6144\n"
        "v_exp_f32 v235, v235\n"
        "v_exp_f32 v236, v236\n"
        "v_exp_f32 v237, v237\n"
        "v_exp_f32 v238, v238\n"
        "v_exp_f32 v239, v239\n"
        "v_exp_f32 v208, v208\n"
        "v_exp_f32 v209, v209\n"
        "v_smfmac_f32_32x32x32_bf16 %1, v[248:251], v[232:239], v252\n"
        "v_exp_f32 v210, v210\n"
        "ds_read_b128 v[248:251], %3 offset:6656\n"
        "v_exp_f32 v211, v211\n"
        "v_exp_f32 v212, v212\n"
        "v_exp_f32 v213, v213\n"
        "v_mfma_f32_32x32x16_bf16 v[224:239], v[240:243], %5, %7\n"
        "v_exp_f32 v214, v214\n"
        "ds_read_b128 v[240:243], %2 offset:7168\n"
        "v_exp_f32 v215, v215\n"
        "v_exp_f32 v216, v216\n"
        "v_exp_f32 v217, v217\n"
        "s_waitcnt lgkmcnt(2)\n"
        "v_smfmac_f32_32x32x32_bf16 %0, v[244:247], v[208:215], v252\n"
        "v_exp_f32 v218, v218\n"
        "v_exp_f32 v219, v219\n"
        "v_exp_f32 v220, v220\n"
        "v_exp_f32 v221, v221\n"
        "v_exp_f32 v222, v222\n"
        "v_exp_f32 v223, v223\n"
        "v_exp_f32 v224, v224\n"
        "v_exp_f32 v225, v225\n"
        "s_waitcnt lgkmcnt(1)\n"
        "v_smfmac_f32_32x32x32_bf16 %0, v[248:251], v[216:223], v252\n"
        "v_exp_f32 v226, v226\n"
        "v_exp_f32 v227, v227\n"
        "v_exp_f32 v228, v228\n"
        "v_exp_f32 v229, v229\n"
        "s_waitcnt lgkmcnt(0)\n"
        "v_mfma_f32_32x32x16_bf16 v[208:223], v[240:243], %4, %6\n"
        "v_exp_f32 v230, v230\n"
        "v_exp_f32 v231, v231\n"
        "v_exp_f32 v232, v232\n"
        "v_exp_f32 v233, v233\n"
        "v_smfmac_f32_32x32x32_bf16 %1, v[244:247], v[224:231], v252\n"
        "v_exp_f32 v234, v234\n"
        "ds_read_b128 v[244:247], %3 offset:7168\n"
        "v_exp_f32 v235, v235\n"
        "v_exp_f32 v236, v236\n"
        "v_exp_f32 v237, v237\n"
        "v_exp_f32 v238, v238\n"
        "v_exp_f32 v239, v239\n"
        "v_exp_f32 v208, v208\n"
        "v_exp_f32 v209, v209\n"
        "v_smfmac_f32_32x32x32_bf16 %1, v[248:251], v[232:239], v252\n"
        "v_exp_f32 v210, v210\n"
        "ds_read_b128 v[248:251], %3 offset:7680\n"
        "v_exp_f32 v211, v211\n"
        "v_exp_f32 v212, v212\n"
        "v_exp_f32 v213, v213\n"
        "v_mfma_f32_32x32x16_bf16 v[224:239], v[240:243], %5, %7\n"
        "v_exp_f32 v214, v214\n"
        "ds_read_b128 v[240:243], %2 offset:8192\n"
        "v_exp_f32 v215, v215\n"
        "v_exp_f32 v216, v216\n"
        "v_exp_f32 v217, v217\n"
        "s_waitcnt lgkmcnt(2)\n"
        "v_smfmac_f32_32x32x32_bf16 %0, v[244:247], v[208:215], v252\n"
        "v_exp_f32 v218, v218\n"
        "v_exp_f32 v219, v219\n"
        "v_exp_f32 v220, v220\n"
        "v_exp_f32 v221, v221\n"
        "v_exp_f32 v222, v222\n"
        "v_exp_f32 v223, v223\n"
        "v_exp_f32 v224, v224\n"
        "v_exp_f32 v225, v225\n"
        "s_waitcnt lgkmcnt(1)\n"
        "v_smfmac_f32_32x32x32_bf16 %0, v[248:251], v[216:223], v252\n"
        "v_exp_f32 v226, v226\n"
        "v_exp_f32 v227, v227\n"
        "v_exp_f32 v228, v228\n"
        "v_exp_f32 v229, v229\n"
        "s_waitcnt lgkmcnt(0)\n"
        "v_mfma_f32_32x32x16_bf16 v[208:223], v[240:243], %4, %6\n"
        "v_exp_f32 v230, v230\n"
        "v_exp_f32 v231, v231\n"
        "v_exp_f32 v232, v232\n"
        "v_exp_f32 v233, v233\n"
        "v_smfmac_f32_32x32x32_bf16 %1, v[244:247], v[224:231], v252\n"
        "v_exp_f32 v234, v234\n"
        "ds_read_b128 v[244:247], %3 offset:8192\n"
        "v_exp_f32 v235, v235\n"
        "v_exp_f32 v236, v236\n"
        "v_exp_f32 v237, v237\n"
        "v_exp_f32 v238, v238\n"
        "v_exp_f32 v239, v239\n"
        "v_add_u32 %2, 8192, %2\n"
        "v_add_u32 %3, %8, %3\n"
        "s_waitcnt vmcnt(0)\n"
        "s_barrier\n"
        ".p2align 6\n"
        "1:\n"
        "v_exp_f32 v208, v208\n"
        "v_exp_f32 v209, v209\n"
        "v_smfmac_f32_32x32x32_bf16 %1, v[248:251], v[232:239], v252\n"
        "v_exp_f32 v210, v210\n"
        "ds_read_b128 v[248:251], %3 offset:512\n"
        "v_exp_f32 v211, v211\n"
        "v_exp_f32 v212, v212\n"
        "v_exp_f32 v213, v213\n"
        "v_mfma_f32_32x32x16_bf16 v[224:239], v[240:243], %5, %7\n"
        "v_exp_f32 v214, v214\n"
        "ds_read_b128 v[240:243], %2 offset:1024\n"
        "v_exp_f32 v215, v215\n"
        "v_exp_f32 v216, v216\n"
        "v_exp_f32 v217, v217\n"
        "s_waitcnt lgkmcnt(2)\n"
        "v_smfmac_f32_32x32x32_bf16 %0, v[244:247], v[208:215], v252\n"
        "v_exp_f32 v218, v218\n"
        "v_exp_f32 v219, v219\n"
        "v_exp_f32 v220, v220\n"
        "v_exp_f32 v221, v221\n"
        "v_exp_f32 v222, v222\n"
        "v_exp_f32 v223, v223\n"
        "v_exp_f32 v224, v224\n"
        "v_exp_f32 v225, v225\n"
        "s_waitcnt lgkmcnt(1)\n"
        "v_smfmac_f32_32x32x32_bf16 %0, v[248:251], v[216:223], v252\n"
        "v_exp_f32 v226, v226\n"
        "v_exp_f32 v227, v227\n"
        "v_exp_f32 v228, v228\n"
        "v_exp_f32 v229, v229\n"
        "s_waitcnt lgkmcnt(0)\n"
        "v_mfma_f32_32x32x16_bf16 v[208:223], v[240:243], %4, %6\n"
        "v_exp_f32 v230, v230\n"
        "v_exp_f32 v231, v231\n"
        "v_exp_f32 v232, v232\n"
        "v_exp_f32 v233, v233\n"
        "v_smfmac_f32_32x32x32_bf16 %1, v[244:247], v[224:231], v252\n"
        "v_exp_f32 v234, v234\n"
        "ds_read_b128 v[244:247], %3 offset:1024\n"
        "v_exp_f32 v235, v235\n"
        "v_exp_f32 v236, v236\n"
        "v_exp_f32 v237, v237\n"
        "v_exp_f32 v238, v238\n"
        "v_exp_f32 v239, v239\n"
        "v_exp_f32 v208, v208\n"
        "v_exp_f32 v209, v209\n"
        "v_smfmac_f32_32x32x32_bf16 %1, v[248:251], v[232:239], v252\n"
        "v_exp_f32 v210, v210\n"
        "ds_read_b128 v[248:251], %3 offset:1536\n"
        "v_exp_f32 v211, v211\n"
        "v_exp_f32 v212, v212\n"
        "v_exp_f32 v213, v213\n"
        "v_mfma_f32_32x32x16_bf16 v[224:239], v[240:243], %5, %7\n"
        "v_exp_f32 v214, v214\n"
        "ds_read_b128 v[240:243], %2 offset:2048\n"
        "v_exp_f32 v215, v215\n"
        "v_exp_f32 v216, v216\n"
        "v_exp_f32 v217, v217\n"
        "s_waitcnt lgkmcnt(2)\n"
        "v_smfmac_f32_32x32x32_bf16 %0, v[244:247], v[208:215], v252\n"
        "v_exp_f32 v218, v218\n"
        "v_exp_f32 v219, v219\n"
        "v_exp_f32 v220, v220\n"
        "v_exp_f32 v221, v221\n"
        "v_exp_f32 v222, v222\n"
        "v_exp_f32 v223, v223\n"
        "v_exp_f32 v224, v224\n"
        "v_exp_f32 v225, v225\n"
        "s_waitcnt lgkmcnt(1)\n"
        "v_smfmac_f32_32x32x32_bf16 %0, v[248:251], v[216:223], v252\n"
        "v_exp_f32 v226, v226\n"
        "v_exp_f32 v227, v227\n"
        "v_exp_f32 v228, v228\n"
        "v_exp_f32 v229, v229\n"
        "s_waitcnt lgkmcnt(0)\n"
        "v_mfma_f32_32x32x16_bf16 v[208:223], v[240:243], %4, %6\n"
        "v_exp_f32 v230, v230\n"
        "v_exp_f32 v231, v231\n"
        "v_exp_f32 v232, v232\n"
        "v_exp_f32 v233, v233\n"
        "v_smfmac_f32_32x32x32_bf16 %1, v[244:247], v[224:231], v252\n"
        "v_exp_f32 v234, v234\n"
        "ds_read_b128 v[244:247], %3 offset:2048\n"
        "v_exp_f32 v235, v235\n"
        "v_exp_f32 v236, v236\n"
        "v_exp_f32 v237, v237\n"
        "v_exp_f32 v238, v238\n"
        "v_exp_f32 v239, v239\n"
        "v_exp_f32 v208, v208\n"
        "v_exp_f32 v209, v209\n"
        "v_smfmac_f32_32x32x32_bf16 %1, v[248:251], v[232:239], v252\n"
        "v_exp_f32 v210, v210\n"
        "ds_read_b128 v[248:251], %3 offset:2560\n"
        "v_exp_f32 v211, v211\n"
        "v_exp_f32 v212, v212\n"
        "v_exp_f32 v213, v213\n"
        "v_mfma_f32_32x32x16_bf16 v[224:239], v[240:243], %5, %7\n"
        "v_exp_f32 v214, v214\n"
        "ds_read_b128 v[240:243], %2 offset:3072\n"
        "v_exp_f32 v215, v215\n"
        "v_exp_f32 v216, v216\n"
        "v_exp_f32 v217, v217\n"
        "s_waitcnt lgkmcnt(2)\n"
        "v_smfmac_f32_32x32x32_bf16 %0, v[244:247], v[208:215], v252\n"
        "v_exp_f32 v218, v218\n"
        "v_exp_f32 v219, v219\n"
        "v_exp_f32 v220, v220\n"
        "v_exp_f32 v221, v221\n"
        "v_exp_f32 v222, v222\n"
        "v_exp_f32 v223, v223\n"
        "v_exp_f32 v224, v224\n"
        "v_exp_f32 v225, v225\n"
        "s_waitcnt lgkmcnt(1)\n"
        "v_smfmac_f32_32x32x32_bf16 %0, v[248:251], v[216:223], v252\n"
        "v_exp_f32 v226, v226\n"
        "v_exp_f32 v227, v227\n"
        "v_exp_f32 v228, v228\n"
        "v_exp_f32 v229, v229\n"
        "s_waitcnt lgkmcnt(0)\n"
        "v_mfma_f32_32x32x16_bf16 v[208:223], v[240:243], %4, %6\n"
        "v_exp_f32 v230, v230\n"
        "v_exp_f32 v231, v231\n"
        "v_exp_f32 v232, v232\n"
        "v_exp_f32 v233, v233\n"
        "v_smfmac_f32_32x32x32_bf16 %1, v[244:247], v[224:231], v252\n"
        "v_exp_f32 v234, v234\n"
        "ds_read_b128 v[244:247], %3 offset:3072\n"
        "v_exp_f32 v235, v235\n"
        "v_exp_f32 v236, v236\n"
        "v_exp_f32 v237, v237\n"
        "v_exp_f32 v238, v238\n"
        "v_exp_f32 v239, v239\n"
        "v_exp_f32 v208, v208\n"
        "v_exp_f32 v209, v209\n"
        "v_smfmac_f32_32x32x32_bf16 %1, v[248:251], v[232:239], v252\n"
        "v_exp_f32 v210, v210\n"
        "ds_read_b128 v[248:251], %3 offset:3584\n"
        "v_exp_f32 v211, v211\n"
        "v_exp_f32 v212, v212\n"
        "v_exp_f32 v213, v213\n"
        "v_mfma_f32_32x32x16_bf16 v[224:239], v[240:243], %5, %7\n"
        "v_exp_f32 v214, v214\n"
        "ds_read_b128 v[240:243], %2 offset:4096\n"
        "v_exp_f32 v215, v215\n"
        "v_exp_f32 v216, v216\n"
        "v_exp_f32 v217, v217\n"
        "s_waitcnt lgkmcnt(2)\n"
        "v_smfmac_f32_32x32x32_bf16 %0, v[244:247], v[208:215], v252\n"
        "v_exp_f32 v218, v218\n"
        "v_exp_f32 v219, v219\n"
        "v_exp_f32 v220, v220\n"
        "v_exp_f32 v221, v221\n"
        "v_exp_f32 v222, v222\n"
        "v_exp_f32 v223, v223\n"
        "v_exp_f32 v224, v224\n"
        "v_exp_f32 v225, v225\n"
        "s_waitcnt lgkmcnt(1)\n"
        "v_smfmac_f32_32x32x32_bf16 %0, v[248:251], v[216:223], v252\n"
        "v_exp_f32 v226, v226\n"
        "v_exp_f32 v227, v227\n"
        "v_exp_f32 v228, v228\n"
        "v_exp_f32 v229, v229\n"
        "s_waitcnt lgkmcnt(0)\n"
        "v_mfma_f32_32x32x16_bf16 v[208:223], v[240:243], %4, %6\n"
        "v_exp_f32 v230, v230\n"
        "v_exp_f32 v231, v231\n"
        "v_exp_f32 v232, v232\n"
        "v_exp_f32 v233, v233\n"
        "v_smfmac_f32_32x32x32_bf16 %1, v[244:247], v[224:231], v252\n"
        "v_exp_f32 v234, v234\n"
        "ds_read_b128 v[244:247], %3 offset:4096\n"
        "v_exp_f32 v235, v235\n"
        "v_exp_f32 v236, v236\n"
        "v_exp_f32 v237, v237\n"
        "v_exp_f32 v238, v238\n"
        "v_exp_f32 v239, v239\n"
        "v_exp_f32 v208, v208\n"
        "v_exp_f32 v209, v209\n"
        "v_smfmac_f32_32x32x32_bf16 %1, v[248:251], v[232:239], v252\n"
        "v_exp_f32 v210, v210\n"
        "ds_read_b128 v[248:251], %3 offset:4608\n"
        "v_exp_f32 v211, v211\n"
        "v_exp_f32 v212, v212\n"
        "v_exp_f32 v213, v213\n"
        "v_mfma_f32_32x32x16_bf16 v[224:239], v[240:243], %5, %7\n"
        "v_exp_f32 v214, v214\n"
        "ds_read_b128 v[240:243], %2 offset:5120\n"
        "v_exp_f32 v215, v215\n"
        "v_exp_f32 v216, v216\n"
        "v_exp_f32 v217, v217\n"
        "s_waitcnt lgkmcnt(2)\n"
        "v_smfmac_f32_32x32x32_bf16 %0, v[244:247], v[208:215], v252\n"
        "v_exp_f32 v218, v218\n"
        "v_exp_f32 v219, v219\n"
        "v_exp_f32 v220, v220\n"
        "v_exp_f32 v221, v221\n"
        "v_exp_f32 v222, v222\n"
        "v_exp_f32 v223, v223\n"
        "v_exp_f32 v224, v224\n"
        "v_exp_f32 v225, v225\n"
        "s_waitcnt lgkmcnt(1)\n"
        "v_smfmac_f32_32x32x32_bf16 %0, v[248:251], v[216:223], v252\n"
        "v_exp_f32 v226, v226\n"
        "v_exp_f32 v227, v227\n"
        "v_exp_f32 v228, v228\n"
        "v_exp_f32 v229, v229\n"
        "s_waitcnt lgkmcnt(0)\n"
        "v_mfma_f32_32x32x16_bf16 v[208:223], v[240:243], %4, %6\n"
        "v_exp_f32 v230, v230\n"
        "v_exp_f32 v231, v231\n"
        "v_exp_f32 v232, v232\n"
        "v_exp_f32 v233, v233\n"
        "v_smfmac_f32_32x32x32_bf16 %1, v[244:247], v[224:231], v252\n"
        "v_exp_f32 v234, v234\n"
        "ds_read_b128 v[244:247], %3 offset:5120\n"
        "v_exp_f32 v235, v235\n"
        "v_exp_f32 v236, v236\n"
        "v_exp_f32 v237, v237\n"
        "v_exp_f32 v238, v238\n"
        "v_exp_f32 v239, v239\n"
        "v_exp_f32 v208, v208\n"
        "v_exp_f32 v209, v209\n"
        "v_smfmac_f32_32x32x32_bf16 %1, v[248:251], v[232:239], v252\n"
        "v_exp_f32 v210, v210\n"
        "ds_read_b128 v[248:251], %3 offset:5632\n"
        "v_exp_f32 v211, v211\n"
        "v_exp_f32 v212, v212\n"
        "v_exp_f32 v213, v213\n"
        "v_mfma_f32_32x32x16_bf16 v[224:239], v[240:243], %5, %7\n"
        "v_exp_f32 v214, v214\n"
        "ds_read_b128 v[240:243], %2 offset:6144\n"
        "v_exp_f32 v215, v215\n"
        "v_exp_f32 v216, v216\n"
        "v_exp_f32 v217, v217\n"
        "s_waitcnt lgkmcnt(2)\n"
        "v_smfmac_f32_32x32x32_bf16 %0, v[244:247], v[208:215], v252\n"
        "v_exp_f32 v218, v218\n"
        "v_exp_f32 v219, v219\n"
        "v_exp_f32 v220, v220\n"
        "v_exp_f32 v221, v221\n"
        "v_exp_f32 v222, v222\n"
        "v_exp_f32 v223, v223\n"
        "v_exp_f32 v224, v224\n"
        "v_exp_f32 v225, v225\n"
        "s_waitcnt lgkmcnt(1)\n"
        "v_smfmac_f32_32x32x32_bf16 %0, v[248:251], v[216:223], v252\n"
        "v_exp_f32 v226, v226\n"
        "v_exp_f32 v227, v227\n"
        "v_exp_f32 v228, v228\n"
        "v_exp_f32 v229, v229\n"
        "s_waitcnt lgkmcnt(0)\n"
        "v_mfma_f32_32x32x16_bf16 v[208:223], v[240:243], %4, %6\n"
        "v_exp_f32 v230, v230\n"
        "v_exp_f32 v231, v231\n"
        "v_exp_f32 v232, v232\n"
        "v_exp_f32 v233, v233\n"
        "v_smfmac_f32_32x32x32_bf16 %1, v[244:247], v[224:231], v252\n"
        "v_exp_f32 v234, v234\n"
        "ds_read_b128 v[244:247], %3 offset:6144\n"
        "v_exp_f32 v235, v235\n"
        "v_exp_f32 v236, v236\n"
        "v_exp_f32 v237, v237\n"
        "v_exp_f32 v238, v238\n"
        "v_exp_f32 v239, v239\n"
        "v_exp_f32 v208, v208\n"
        "v_exp_f32 v209, v209\n"
        "v_smfmac_f32_32x32x32_bf16 %1, v[248:251], v[232:239], v252\n"
        "v_exp_f32 v210, v210\n"
        "ds_read_b128 v[248:251], %3 offset:6656\n"
        "v_exp_f32 v211, v211\n"
        "v_exp_f32 v212, v212\n"
        "v_exp_f32 v213, v213\n"
        "v_mfma_f32_32x32x16_bf16 v[224:239], v[240:243], %5, %7\n"
        "v_exp_f32 v214, v214\n"
        "ds_read_b128 v[240:243], %2 offset:7168\n"
        "v_exp_f32 v215, v215\n"
        "v_exp_f32 v216, v216\n"
        "v_exp_f32 v217, v217\n"
        "s_waitcnt lgkmcnt(2)\n"
        "v_smfmac_f32_32x32x32_bf16 %0, v[244:247], v[208:215], v252\n"
        "v_exp_f32 v218, v218\n"
        "v_exp_f32 v219, v219\n"
        "v_exp_f32 v220, v220\n"
        "v_exp_f32 v221, v221\n"
        "v_exp_f32 v222, v222\n"
        "v_exp_f32 v223, v223\n"
        "v_exp_f32 v224, v224\n"
        "v_exp_f32 v225, v225\n"
        "s_waitcnt lgkmcnt(1)\n"
        "v_smfmac_f32_32x32x32_bf16 %0, v[248:251], v[216:223], v252\n"
        "v_exp_f32 v226, v226\n"
        "v_exp_f32 v227, v227\n"
        "v_exp_f32 v228, v228\n"
        "v_exp_f32 v229, v229\n"
        "s_waitcnt lgkmcnt(0)\n"
        "v_mfma_f32_32x32x16_bf16 v[208:223], v[240:243], %4, %6\n"
        "v_exp_f32 v230, v230\n"
        "v_exp_f32 v231, v231\n"
        "v_exp_f32 v232, v232\n"
        "v_exp_f32 v233, v233\n"
        "v_smfmac_f32_32x32x32_bf16 %1, v[244:247], v[224:231], v252\n"
        "v_exp_f32 v234, v234\n"
        "ds_read_b128 v[244:247], %3 offset:7168\n"
        "v_exp_f32 v235, v235\n"
        "v_exp_f32 v236, v236\n"
        "v_exp_f32 v237, v237\n"
        "v_exp_f32 v238, v238\n"
        "v_exp_f32 v239, v239\n"
        "v_exp_f32 v208, v208\n"
        "v_exp_f32 v209, v209\n"
        "v_smfmac_f32_32x32x32_bf16 %1, v[248:251], v[232:239], v252\n"
        "v_exp_f32 v210, v210\n"
        "ds_read_b128 v[248:251], %3 offset:7680\n"
        "v_exp_f32 v211, v211\n"
        "v_exp_f32 v212, v212\n"
        "v_exp_f32 v213, v213\n"
        "v_mfma_f32_32x32x16_bf16 v[224:239], v[240:243], %5, %7\n"
        "v_exp_f32 v214, v214\n"
        "ds_read_b128 v[240:243], %2 offset:8192\n"
        "v_exp_f32 v215, v215\n"
        "v_exp_f32 v216, v216\n"
        "v_exp_f32 v217, v217\n"
        "s_waitcnt lgkmcnt(2)\n"
        "v_smfmac_f32_32x32x32_bf16 %0, v[244:247], v[208:215], v252\n"
        "v_exp_f32 v218, v218\n"
        "v_exp_f32 v219, v219\n"
        "v_exp_f32 v220, v220\n"
        "v_exp_f32 v221, v221\n"
        "v_exp_f32 v222, v222\n"
        "v_exp_f32 v223, v223\n"
        "v_exp_f32 v224, v224\n"
        "v_exp_f32 v225, v225\n"
        "s_waitcnt lgkmcnt(1)\n"
        "v_smfmac_f32_32x32x32_bf16 %0, v[248:251], v[216:223], v252\n"
        "v_exp_f32 v226, v226\n"
        "v_exp_f32 v227, v227\n"
        "v_exp_f32 v228, v228\n"
        "v_exp_f32 v229, v229\n"
        "s_waitcnt lgkmcnt(0)\n"
        "v_mfma_f32_32x32x16_bf16 v[208:223], v[240:243], %4, %6\n"
        "v_exp_f32 v230, v230\n"
        "v_exp_f32 v231, v231\n"
        "v_exp_f32 v232, v232\n"
        "v_exp_f32 v233, v233\n"
        "v_smfmac_f32_32x32x32_bf16 %1, v[244:247], v[224:231], v252\n"
        "v_exp_f32 v234, v234\n"
        "ds_read_b128 v[244:247], %3 offset:8192\n"
        "v_exp_f32 v235, v235\n"
        "v_exp_f32 v236, v236\n"
        "v_exp_f32 v237, v237\n"
        "v_exp_f32 v238, v238\n"
        "v_exp_f32 v239, v239\n"
        "v_add_u32 %2, 8192, %2\n"
        "v_add_u32 %3, %8, %3\n"
        "s_sub_u32 s40, s40, 1\n"
        "s_cmp_lg_u32 s40, 0\n"
        "s_cbranch_scc1 1b\n"
        "s_barrier\n"
        "s_mov_b64 s[42:43], %9\n"
        "s_mov_b32 s44, %10\n"
        "v_exp_f32 v208, v208\n"
        "v_exp_f32 v209, v209\n"
        "v_smfmac_f32_32x32x32_bf16 %1, v[248:251], v[232:239], v252\n"
        "v_exp_f32 v210, v210\n"
        "ds_read_b128 v[248:251], %3 offset:512\n"
        "v_exp_f32 v211, v211\n"
        "v_exp_f32 v212, v212\n"
        "v_exp_f32 v213, v213\n"
        "v_mfma_f32_32x32x16_bf16 v[224:239], v[240:243], %5, %7\n"
        "v_exp_f32 v214, v214\n"
        "ds_read_b128 v[240:243], %2 offset:1024\n"
        "v_exp_f32 v215, v215\n"
        "v_exp_f32 v216, v216\n"
        "v_exp_f32 v217, v217\n"
        "s_waitcnt lgkmcnt(2)\n"
        "v_smfmac_f32_32x32x32_bf16 %0, v[244:247], v[208:215], v252\n"
        "v_exp_f32 v218, v218\n"
        "v_exp_f32 v219, v219\n"
        "s_mov_b32 m0, s44\n"
        "s_add_u32 s44, s44, 1024\n"
        "global_load_lds_dwordx4 %12, s[42:43]\n"
        "s_add_u32 s42, s42, 8192\n"
        "s_addc_u32 s43, s43, 0\n"
        "v_exp_f32 v220, v220\n"
        "v_exp_f32 v221, v221\n"
        "v_exp_f32 v222, v222\n"
        "s_mov_b32 m0, s44\n"
        "s_add_u32 s44, s44, 1024\n"
        "global_load_lds_dwordx4 %12, s[42:43]\n"
        "s_add_u32 s42, s42, 8192\n"
        "s_addc_u32 s43, s43, 0\n"
        "v_exp_f32 v223, v223\n"
        "v_exp_f32 v224, v224\n"
        "v_exp_f32 v225, v225\n"
        "s_waitcnt lgkmcnt(1)\n"
        "v_smfmac_f32_32x32x32_bf16 %0, v[248:251], v[216:223], v252\n"
        "v_exp_f32 v226, v226\n"
        "v_exp_f32 v227, v227\n"
        "v_exp_f32 v228, v228\n"
        "v_exp_f32 v229, v229\n"
        "s_waitcnt lgkmcnt(0)\n"
        "v_mfma_f32_32x32x16_bf16 v[208:223], v[240:243], %4, %6\n"
        "v_exp_f32 v230, v230\n"
        "v_exp_f32 v231, v231\n"
        "v_exp_f32 v232, v232\n"
        "v_exp_f32 v233, v233\n"
        "v_smfmac_f32_32x32x32_bf16 %1, v[244:247], v[224:231], v252\n"
        "v_exp_f32 v234, v234\n"
        "ds_read_b128 v[244:247], %3 offset:1024\n"
        "v_exp_f32 v235, v235\n"
        "s_mov_b32 m0, s44\n"
        "s_add_u32 s44, s44, 1024\n"
        "global_load_lds_dwordx4 %12, s[42:43]\n"
        "s_add_u32 s42, s42, 8192\n"
        "s_addc_u32 s43, s43, 0\n"
        "v_exp_f32 v236, v236\n"
        "v_exp_f32 v237, v237\n"
        "v_exp_f32 v238, v238\n"
        "s_mov_b32 m0, s44\n"
        "s_add_u32 s44, s44, 1024\n"
        "global_load_lds_dwordx4 %12, s[42:43]\n"
        "s_add_u32 s42, s42, 8192\n"
        "s_addc_u32 s43, s43, 0\n"
        "v_exp_f32 v239, v239\n"
        "v_exp_f32 v208, v208\n"
        "v_exp_f32 v209, v209\n"
        "v_smfmac_f32_32x32x32_bf16 %1, v[248:251], v[232:239], v252\n"
        "v_exp_f32 v210, v210\n"
        "ds_read_b128 v[248:251], %3 offset:1536\n"
        "v_exp_f32 v211, v211\n"
        "v_exp_f32 v212, v212\n"
        "v_exp_f32 v213, v213\n"
        "v_mfma_f32_32x32x16_bf16 v[224:239], v[240:243], %5, %7\n"
        "v_exp_f32 v214, v214\n"
        "ds_read_b128 v[240:243], %2 offset:2048\n"
        "v_exp_f32 v215, v215\n"
        "v_exp_f32 v216, v216\n"
        "v_exp_f32 v217, v217\n"
        "s_waitcnt lgkmcnt(2)\n"
        "v_smfmac_f32_32x32x32_bf16 %0, v[244:247], v[208:215], v252\n"
        "v_exp_f32 v218, v218\n"
        "v_exp_f32 v219, v219\n"
        "s_mov_b32 m0, s44\n"
        "s_add_u32 s44, s44, 1024\n"
        "global_load_lds_dwordx4 %12, s[42:43]\n"
        "s_add_u32 s42, s42, 8192\n"
        "s_addc_u32 s43, s43, 0\n"
        "v_exp_f32 v220, v220\n"
        "v_exp_f32 v221, v221\n"
        "v_exp_f32 v222, v222\n"
        "s_mov_b32 m0, s44\n"
        "s_add_u32 s44, s44, 1024\n"
        "global_load_lds_dwordx4 %12, s[42:43]\n"
        "s_add_u32 s42, s42, 8192\n"
        "s_addc_u32 s43, s43, 0\n"
        "v_exp_f32 v223, v223\n"
        "v_exp_f32 v224, v224\n"
        "v_exp_f32 v225, v225\n"
        "s_waitcnt lgkmcnt(1)\n"
        "v_smfmac_f32_32x32x32_bf16 %0, v[248:251], v[216:223], v252\n"
        "v_exp_f32 v226, v226\n"
        "v_exp_f32 v227, v227\n"
        "v_exp_f32 v228, v228\n"
        "v_exp_f32 v229, v229\n"
        "s_waitcnt lgkmcnt(0)\n"
        "v_mfma_f32_32x32x16_bf16 v[208:223], v[240:243], %4, %6\n"
        "v_exp_f32 v230, v230\n"
        "v_exp_f32 v231, v231\n"
        "v_exp_f32 v232, v232\n"
        "v_exp_f32 v233, v233\n"
        "v_smfmac_f32_32x32x32_bf16 %1, v[244:247], v[224:231], v252\n"
        "v_exp_f32 v234, v234\n"
        "ds_read_b128 v[244:247], %3 offset:2048\n"
        "v_exp_f32 v235, v235\n"
        "s_mov_b32 m0, s44\n"
        "s_add_u32 s44, s44, 1024\n"
        "global_load_lds_dwordx4 %12, s[42:43]\n"
        "s_add_u32 s42, s42, 8192\n"
        "s_addc_u32 s43, s43, 0\n"
        "v_exp_f32 v236, v236\n"
        "v_exp_f32 v237, v237\n"
        "v_exp_f32 v238, v238\n"
        "s_mov_b32 m0, s44\n"
        "s_add_u32 s44, s44, 1024\n"
        "global_load_lds_dwordx4 %12, s[42:43]\n"
        "s_add_u32 s42, s42, 8192\n"
        "s_addc_u32 s43, s43, 0\n"
        "v_exp_f32 v239, v239\n"
        "v_exp_f32 v208, v208\n"
        "v_exp_f32 v209, v209\n"
        "v_smfmac_f32_32x32x32_bf16 %1, v[248:251], v[232:239], v252\n"
        "v_exp_f32 v210, v210\n"
        "ds_read_b128 v[248:251], %3 offset:2560\n"
        "v_exp_f32 v211, v211\n"
        "v_exp_f32 v212, v212\n"
        "v_exp_f32 v213, v213\n"
        "v_mfma_f32_32x32x16_bf16 v[224:239], v[240:243], %5, %7\n"
        "v_exp_f32 v214, v214\n"
        "ds_read_b128 v[240:243], %2 offset:3072\n"
        "v_exp_f32 v215, v215\n"
        "v_exp_f32 v216, v216\n"
        "v_exp_f32 v217, v217\n"
        "s_waitcnt lgkmcnt(2)\n"
        "v_smfmac_f32_32x32x32_bf16 %0, v[244:247], v[208:215], v252\n"
        "v_exp_f32 v218, v218\n"
        "v_exp_f32 v219, v219\n"
        "s_mov_b32 s44, %11\n"
        "s_mov_b32 m0, s44\n"
        "s_add_u32 s44, s44, 1024\n"
        "global_load_lds_dwordx4 %12, s[42:43]\n"
        "s_add_u32 s42, s42, 8192\n"
        "s_addc_u32 s43, s43, 0\n"
        "v_exp_f32 v220, v220\n"
        "v_exp_f32 v221, v221\n"
        "v_exp_f32 v222, v222\n"
        "s_mov_b32 m0, s44\n"
        "s_add_u32 s44, s44, 1024\n"
        "global_load_lds_dwordx4 %12, s[42:43]\n"
        "s_add_u32 s42, s42, 8192\n"
        "s_addc_u32 s43, s43, 0\n"
        "v_exp_f32 v223, v223\n"
        "v_exp_f32 v224, v224\n"
        "v_exp_f32 v225, v225\n"
        "s_waitcnt lgkmcnt(1)\n"
        "v_smfmac_f32_32x32x32_bf16 %0, v[248:251], v[216:223], v252\n"
        "v_exp_f32 v226, v226\n"
        "v_exp_f32 v227, v227\n"
        "v_exp_f32 v228, v228\n"
        "v_exp_f32 v229, v229\n"
        "s_waitcnt lgkmcnt(0)\n"
        "v_mfma_f32_32x32x16_bf16 v[208:223], v[240:243], %4, %6\n"
        "v_exp_f32 v230, v230\n"
        "v_exp_f32 v231, v231\n"
        "v_exp_f32 v232, v232\n"
        "v_exp_f32 v233, v233\n"
        "v_smfmac_f32_32x32x32_bf16 %1, v[244:247], v[224:231], v252\n"
        "v_exp_f32 v234, v234\n"
        "ds_read_b128 v[244:247], %3 offset:3072\n"
        "v_exp_f32 v235, v235\n"
        "s_mov_b32 m0, s44\n"
        "s_add_u32 s44, s44, 1024\n"
        "global_load_lds_dwordx4 %12, s[42:43]\n"
        "s_add_u32 s42, s42, 8192\n"
        "s_addc_u32 s43, s43, 0\n"
        "v_exp_f32 v236, v236\n"
        "v_exp_f32 v237, v237\n"
        "v_exp_f32 v238, v238\n"
        "s_mov_b32 m0, s44\n"
        "s_add_u32 s44, s44, 1024\n"
        "global_load_lds_dwordx4 %12, s[42:43]\n"
        "s_add_u32 s42, s42, 8192\n"
        "s_addc_u32 s43, s43, 0\n"
        "v_exp_f32 v239, v239\n"
        "v_exp_f32 v208, v208\n"
        "v_exp_f32 v209, v209\n"
        "v_smfmac_f32_32x32x32_bf16 %1, v[248:251], v[232:239], v252\n"
        "v_exp_f32 v210, v210\n"
        "ds_read_b128 v[248:251], %3 offset:3584\n"
        "v_exp_f32 v211, v211\n"
        "v_exp_f32 v212, v212\n"
        "v_exp_f32 v213, v213\n"
        "v_mfma_f32_32x32x16_bf16 v[224:239], v[240:243], %5, %7\n"
        "v_exp_f32 v214, v214\n"
        "ds_read_b128 v[240:243], %2 offset:4096\n"
        "v_exp_f32 v215, v215\n"
        "v_exp_f32 v216, v216\n"
        "v_exp_f32 v217, v217\n"
        "s_waitcnt lgkmcnt(2)\n"
        "v_smfmac_f32_32x32x32_bf16 %0, v[244:247], v[208:215], v252\n"
        "v_exp_f32 v218, v218\n"
        "v_exp_f32 v219, v219\n"
        "s_mov_b32 m0, s44\n"
        "s_add_u32 s44, s44, 1024\n"
        "global_load_lds_dwordx4 %12, s[42:43]\n"
        "s_add_u32 s42, s42, 8192\n"
        "s_addc_u32 s43, s43, 0\n"
        "v_exp_f32 v220, v220\n"
        "v_exp_f32 v221, v221\n"
        "v_exp_f32 v222, v222\n"
        "s_mov_b32 m0, s44\n"
        "s_add_u32 s44, s44, 1024\n"
        "global_load_lds_dwordx4 %12, s[42:43]\n"
        "s_add_u32 s42, s42, 8192\n"
        "s_addc_u32 s43, s43, 0\n"
        "v_exp_f32 v223, v223\n"
        "v_exp_f32 v224, v224\n"
        "v_exp_f32 v225, v225\n"
        "s_waitcnt lgkmcnt(1)\n"
        "v_smfmac_f32_32x32x32_bf16 %0, v[248:251], v[216:223], v252\n"
        "v_exp_f32 v226, v226\n"
        "v_exp_f32 v227, v227\n"
        "v_exp_f32 v228, v228\n"
        "v_exp_f32 v229, v229\n"
        "s_waitcnt lgkmcnt(0)\n"
        "v_mfma_f32_32x32x16_bf16 v[208:223], v[240:243], %4, %6\n"
        "v_exp_f32 v230, v230\n"
        "v_exp_f32 v231, v231\n"
        "v_exp_f32 v232, v232\n"
        "v_exp_f32 v233, v233\n"
        "v_smfmac_f32_32x32x32_bf16 %1, v[244:247], v[224:231], v252\n"
        "v_exp_f32 v234, v234\n"
        "ds_read_b128 v[244:247], %3 offset:4096\n"
        "v_exp_f32 v235, v235\n"
        "s_mov_b32 m0, s44\n"
        "s_add_u32 s44, s44, 1024\n"
        "global_load_lds_dwordx4 %12, s[42:43]\n"
        "s_add_u32 s42, s42, 8192\n"
        "s_addc_u32 s43, s43, 0\n"
        "v_exp_f32 v236, v236\n"
        "v_exp_f32 v237, v237\n"
        "v_exp_f32 v238, v238\n"
        "s_mov_b32 m0, s44\n"
        "s_add_u32 s44, s44, 1024\n"
        "global_load_lds_dwordx4 %12, s[42:43]\n"
        "s_add_u32 s42, s42, 8192\n"
        "s_addc_u32 s43, s43, 0\n"
        "v_exp_f32 v239, v239\n"
        "v_exp_f32 v208, v208\n"
        "v_exp_f32 v209, v209\n"
        "v_smfmac_f32_32x32x32_bf16 %1, v[248:251], v[232:239], v252\n"
        "v_exp_f32 v210, v210\n"
        "ds_read_b128 v[248:251], %3 offset:4608\n"
        "v_exp_f32 v211, v211\n"
        "v_exp_f32 v212, v212\n"
        "v_exp_f32 v213, v213\n"
        "v_mfma_f32_32x32x16_bf16 v[224:239], v[240:243], %5, %7\n"
        "v_exp_f32 v214, v214\n"
        "ds_read_b128 v[240:243], %2 offset:5120\n"
        "v_exp_f32 v215, v215\n"
        "v_exp_f32 v216, v216\n"
        "v_exp_f32 v217, v217\n"
        "s_waitcnt lgkmcnt(2)\n"
        "v_smfmac_f32_32x32x32_bf16 %0, v[244:247], v[208:215], v252\n"
        "v_exp_f32 v218, v218\n"
        "v_exp_f32 v219, v219\n"
        "v_exp_f32 v220, v220\n"
        "v_exp_f32 v221, v221\n"
        "v_exp_f32 v222, v222\n"
        "v_exp_f32 v223, v223\n"
        "v_exp_f32 v224, v224\n"
        "v_exp_f32 v225, v225\n"
        "s_waitcnt lgkmcnt(1)\n"
        "v_smfmac_f32_32x32x32_bf16 %0, v[248:251], v[216:223], v252\n"
        "v_exp_f32 v226, v226\n"
        "v_exp_f32 v227, v227\n"
        "v_exp_f32 v228, v228\n"
        "v_exp_f32 v229, v229\n"
        "s_waitcnt lgkmcnt(0)\n"
        "v_mfma_f32_32x32x16_bf16 v[208:223], v[240:243], %4, %6\n"
        "v_exp_f32 v230, v230\n"
        "v_exp_f32 v231, v231\n"
        "v_exp_f32 v232, v232\n"
        "v_exp_f32 v233, v233\n"
        "v_smfmac_f32_32x32x32_bf16 %1, v[244:247], v[224:231], v252\n"
        "v_exp_f32 v234, v234\n"
        "ds_read_b128 v[244:247], %3 offset:5120\n"
        "v_exp_f32 v235, v235\n"
        "v_exp_f32 v236, v236\n"
        "v_exp_f32 v237, v237\n"
        "v_exp_f32 v238, v238\n"
        "v_exp_f32 v239, v239\n"
        "v_exp_f32 v208, v208\n"
        "v_exp_f32 v209, v209\n"
        "v_smfmac_f32_32x32x32_bf16 %1, v[248:251], v[232:239], v252\n"
        "v_exp_f32 v210, v210\n"
        "ds_read_b128 v[248:251], %3 offset:5632\n"
        "v_exp_f32 v211, v211\n"
        "v_exp_f32 v212, v212\n"
        "v_exp_f32 v213, v213\n"
        "v_mfma_f32_32x32x16_bf16 v[224:239], v[240:243], %5, %7\n"
        "v_exp_f32 v214, v214\n"
        "ds_read_b128 v[240:243], %2 offset:6144\n"
        "v_exp_f32 v215, v215\n"
        "v_exp_f32 v216, v216\n"
        "v_exp_f32 v217, v217\n"
        "s_waitcnt lgkmcnt(2)\n"
        "v_smfmac_f32_32x32x32_bf16 %0, v[244:247], v[208:215], v252\n"
        "v_exp_f32 v218, v218\n"
        "v_exp_f32 v219, v219\n"
        "v_exp_f32 v220, v220\n"
        "v_exp_f32 v221, v221\n"
        "v_exp_f32 v222, v222\n"
        "v_exp_f32 v223, v223\n"
        "v_exp_f32 v224, v224\n"
        "v_exp_f32 v225, v225\n"
        "s_waitcnt lgkmcnt(1)\n"
        "v_smfmac_f32_32x32x32_bf16 %0, v[248:251], v[216:223], v252\n"
        "v_exp_f32 v226, v226\n"
        "v_exp_f32 v227, v227\n"
        "v_exp_f32 v228, v228\n"
        "v_exp_f32 v229, v229\n"
        "s_waitcnt lgkmcnt(0)\n"
        "v_mfma_f32_32x32x16_bf16 v[208:223], v[240:243], %4, %6\n"
        "v_exp_f32 v230, v230\n"
        "v_exp_f32 v231, v231\n"
        "v_exp_f32 v232, v232\n"
        "v_exp_f32 v233, v233\n"
        "v_smfmac_f32_32x32x32_bf16 %1, v[244:247], v[224:231], v252\n"
        "v_exp_f32 v234, v234\n"
        "ds_read_b128 v[244:247], %3 offset:6144\n"
        "v_exp_f32 v235, v235\n"
        "v_exp_f32 v236, v236\n"
        "v_exp_f32 v237, v237\n"
        "v_exp_f32 v238, v238\n"
        "v_exp_f32 v239, v239\n"
        "v_exp_f32 v208, v208\n"
        "v_exp_f32 v209, v209\n"
        "v_smfmac_f32_32x32x32_bf16 %1, v[248:251], v[232:239], v252\n"
        "v_exp_f32 v210, v210\n"
        "ds_read_b128 v[248:251], %3 offset:6656\n"
        "v_exp_f32 v211, v211\n"
        "v_exp_f32 v212, v212\n"
        "v_exp_f32 v213, v213\n"
        "v_mfma_f32_32x32x16_bf16 v[224:239], v[240:243], %5, %7\n"
        "v_exp_f32 v214, v214\n"
        "ds_read_b128 v[240:243], %2 offset:7168\n"
        "v_exp_f32 v215, v215\n"
        "v_exp_f32 v216, v216\n"
        "v_exp_f32 v217, v217\n"
        "s_waitcnt lgkmcnt(2)\n"
        "v_smfmac_f32_32x32x32_bf16 %0, v[244:247], v[208:215], v252\n"
        "v_exp_f32 v218, v218\n"
        "v_exp_f32 v219, v219\n"
        "v_exp_f32 v220, v220\n"
        "v_exp_f32 v221, v221\n"
        "v_exp_f32 v222, v222\n"
        "v_exp_f32 v223, v223\n"
        "v_exp_f32 v224, v224\n"
        "v_exp_f32 v225, v225\n"
        "s_waitcnt lgkmcnt(1)\n"
        "v_smfmac_f32_32x32x32_bf16 %0, v[248:251], v[216:223], v252\n"
        "v_exp_f32 v226, v226\n"
        "v_exp_f32 v227, v227\n"
        "v_exp_f32 v228, v228\n"
        "v_exp_f32 v229, v229\n"
        "s_waitcnt lgkmcnt(0)\n"
        "v_mfma_f32_32x32x16_bf16 v[208:223], v[240:243], %4, %6\n"
        "v_exp_f32 v230, v230\n"
        "v_exp_f32 v231, v231\n"
        "v_exp_f32 v232, v232\n"
        "v_exp_f32 v233, v233\n"
        "v_smfmac_f32_32x32x32_bf16 %1, v[244:247], v[224:231], v252\n"
        "v_exp_f32 v234, v234\n"
        "ds_read_b128 v[244:247], %3 offset:7168\n"
        "v_exp_f32 v235, v235\n"
        "v_exp_f32 v236, v236\n"
        "v_exp_f32 v237, v237\n"
        "v_exp_f32 v238, v238\n"
        "v_exp_f32 v239, v239\n"
        "v_exp_f32 v208, v208\n"
        "v_exp_f32 v209, v209\n"
        "v_smfmac_f32_32x32x32_bf16 %1, v[248:251], v[232:239], v252\n"
        "v_exp_f32 v210, v210\n"
        "ds_read_b128 v[248:251], %3 offset:7680\n"
        "v_exp_f32 v211, v211\n"
        "v_exp_f32 v212, v212\n"
        "v_exp_f32 v213, v213\n"
        "v_mfma_f32_32x32x16_bf16 v[224:239], v[240:243], %5, %7\n"
        "v_exp_f32 v214, v214\n"
        "ds_read_b128 v[240:243], %2 offset:8192\n"
        "v_exp_f32 v215, v215\n"
        "v_exp_f32 v216, v216\n"
        "v_exp_f32 v217, v217\n"
        "s_waitcnt lgkmcnt(2)\n"
        "v_smfmac_f32_32x32x32_bf16 %0, v[244:247], v[208:215], v252\n"
        "v_exp_f32 v218, v218\n"
        "v_exp_f32 v219, v219\n"
        "v_exp_f32 v220, v220\n"
        "v_exp_f32 v221, v221\n"
        "v_exp_f32 v222, v222\n"
        "v_exp_f32 v223, v223\n"
        "v_exp_f32 v224, v224\n"
        "v_exp_f32 v225, v225\n"
        "s_waitcnt lgkmcnt(1)\n"
        "v_smfmac_f32_32x32x32_bf16 %0, v[248:251], v[216:223], v252\n"
        "v_exp_f32 v226, v226\n"
        "v_exp_f32 v227, v227\n"
        "v_exp_f32 v228, v228\n"
        "v_exp_f32 v229, v229\n"
        "s_waitcnt lgkmcnt(0)\n"
        "v_mfma_f32_32x32x16_bf16 v[208:223], v[240:243], %4, %6\n"
        "v_exp_f32 v230, v230\n"
        "v_exp_f32 v231, v231\n"
        "v_exp_f32 v232, v232\n"
        "v_exp_f32 v233, v233\n"
        "v_smfmac_f32_32x32x32_bf16 %1, v[244:247], v[224:231], v252\n"
        "v_exp_f32 v234, v234\n"
        "ds_read_b128 v[244:247], %3 offset:8192\n"
        "v_exp_f32 v235, v235\n"
        "v_exp_f32 v236, v236\n"
        "v_exp_f32 v237, v237\n"
        "v_exp_f32 v238, v238\n"
        "v_exp_f32 v239, v239\n"
        "s_mov_b32 m0, s41\n"
        "s_nop 1\n"
        "v_smfmac_f32_32x32x32_bf16 %1, v[248:251], v[232:239], v252\n"
        "s_waitcnt lgkmcnt(0)\n"
        "s_nop 15\n"
        "s_nop 3\n"
        : "+v"(oa), "+v"(ob), "+v"(aK), "+v"(aV)
        : "v"(qa), "v"(qb), "v"(ca), "v"(cb), "v"(incV), "s"(wo), "s"(seg0), "s"(seg1), "v"(wo_lane_off(lane)),
          "s"(kv_rest), "s"(lds_rest), "v"(lane * 16)
        : "v208", "v209", "v210", "v211", "v212", "v213", "v214", "v215", "v216", "v217", "v218", "v219", "v220", "v221", "v222", "v223", "v224", "v225", "v226", "v227", "v228", "v229", "v230", "v231", "v232", "v233", "v234", "v235", "v236", "v237", "v238", "v239", "v240", "v241", "v242", "v243", "v244", "v245", "v246", "v247", "v248", "v249", "v250", "v251", "v252", "s40", "s41", "s42", "s43", "s44", "scc", "memory");
}

__global__ __launch_bounds__(NTHR, 2) void mqa_kernel(const char* __restrict__ Ax, const char* __restrict__ Awq, const char* __restrict__ Awk,
                                                      const char* __restrict__ Awv, const char* __restrict__ Awo,
                                                      const char* __restrict__ Bwq, const char* __restrict__ Bwk, const char* __restrict__ Bwv,
                                                      const char* __restrict__ Bwo, const char* __restrict__ gate_w, char* __restrict__ out, char* ws) {
    __shared__ __attribute__((aligned(16))) char smem[LDS_BYTES];
    const unsigned lds = (unsigned)(size_t)(LAS char*)smem;
    asm volatile(".Lmqa_begin:");
    const Ids d = make_ids();
    const int l = d.lane;

    const char* wq = d.b ? Bwq : Awq;
    unsigned epoch = 0;
    u32x2 own_k = {0u, 0u};
    if (d.w == 0) {
        dma_lo(d, lds, Ax, d.b ? Bwk : Awk, d.b ? Bwv : Awv);
        asm volatile("global_load_dword %0, %1, %2 sc1\n\ts_waitcnt vmcnt(1)\n\ts_barrier" : "=&v"(epoch) : "v"(0u), "s"(ws + WS_EPOCH) : "memory");
        phase_kv(d, lds, ws, own_k);
    } else if (d.w == 1) {
        u32x4 gw[16];
#pragma unroll
        for (int s = 0; s < 16; ++s) gw[s] = *(const GAS u32x4*)(gate_w + s * 64 + (l >> 4) * 16);
        dma_lo(d, lds, Ax, d.b ? Bwk : Awk, d.b ? Bwv : Awv);
        asm volatile("s_waitcnt vmcnt(0)\n\ts_barrier" ::: "memory");
        phase_gate(d, lds, ws, gw);
        asm volatile("global_load_dword %0, %1, %2 sc1" : "=&v"(epoch) : "v"(0u), "s"(ws + WS_EPOCH) : "memory");
    } else if (d.w < 4) {
        dma_lo(d, lds, Ax, d.b ? Bwk : Awk, d.b ? Bwv : Awv);
        asm volatile("s_waitcnt vmcnt(0)\n\ts_barrier" ::: "memory");
    } else {
        asm volatile("s_lshl_b32 s44, %2, 12\n\ts_cmp_lt_u32 s44, (.Lmqa_end - .Lmqa_begin)\n\ts_cbranch_scc0 1f\n\t"
                     "s_min_u32 s44, s44, (.Lmqa_end - .Lmqa_begin - 4096)\n\t"
                     "s_getpc_b64 s[42:43]\n.Lmqa_here:\n\ts_sub_u32 s42, s42, (.Lmqa_here - .Lmqa_begin)\n\ts_subb_u32 s43, s43, 0\n\t"
                     "s_add_u32 s42, s42, s44\n\ts_addc_u32 s43, s43, 0\n\tglobal_load_dword %0, %1, s[42:43]\n1:"
                     : "+v"(epoch) : "v"(l * 64), "s"((blockIdx.x >> 3) * 4 + (d.w - 4)) : "s42", "s43", "s44", "scc", "memory");
    }

    u32x4 qa, qb;
    u32x2 flags;
    phase_q(d, lds, Ax, wq, ws, epoch, qa, qb, flags);
    const unsigned tag = __builtin_amdgcn_readfirstlane(epoch) + 1u;
    if (d.w == 0 && !__all(flags[0] == tag && flags[1] == tag))
        poll2(ws + WS_KVF + d.b * KVF_BRANCH, kvf_off(2 * l), tag);
    WAIT_VM_BAR(0);

    const char* kv = ws + WS_KV + d.b * 131072;
    dma_kv_first(d, lds, kv);
    if (d.w == 7) {
        glds4_sc1(kv + (blockIdx.x >> 3) * 4096, l * 64, lds + L_TOUCH);
    }
    init_const_rows(lds);
    if (threadIdx.x == 128) *(LAS unsigned*)(size_t)(lds + L_REDO) = 0u;
    if (d.w == 0) {
        const int kg = l >> 4;
        *(LAS u32x2*)(size_t)(lds + L_OWNK + ((kg & 1) * 16 + (l & 15)) * 16 + (kg >> 1) * 8) = own_k;
    }
    asm volatile("s_waitcnt lgkmcnt(0)\n\ts_barrier" ::: "memory");

    float ma, mb;
    {
        const bf16x8 k0 = as_bf(lds_read16(lds + L_OWNK + ((l >> 5) * 16 + (l & 15)) * 16));
        f32x16 z;
#pragma unroll
        for (int i = 0; i < 16; ++i) z[i] = 0.f;
        ma = half_swap_max(max16(MFMA32(k0, as_bf(qa), z)));
        mb = half_swap_max(max16(MFMA32(k0, as_bf(qb), z)));
    }
    const char* wo_src = ((d.w >> 2) ? Bwo : Awo) + (size_t)((d.w & 3) * 128) * 1024 + d.sub * 128;
    const unsigned seg0 = wo_seg(lds, 2 * d.w), seg1 = wo_seg(lds, 2 * d.w + 1);
    const char* o_dst = ws + WS_O + (size_t)((d.b * 64 + d.rb * 2 + (d.w & 1)) * 32 + d.hg * 8 + (d.w >> 1) * 2) * 1024;
#pragma clang loop unroll(disable)
    for (int pass = 0;; ++pass) {
        f32x16 oa, ob;
        attn_pass(lds, l, qa, qb, ma, mb, kv + kv_rest_off(d), lds + kv_rest_off(d), wo_src, seg0, seg1, oa, ob);
        if (__any(!(oa[8] < 1.8446744e19f) || !(ob[8] < 1.8446744e19f))) *(volatile LAS unsigned*)(size_t)(lds + L_REDO) = 1u;
        store_o(o_dst, l, oa, ob);
        if (blockIdx.x == 0 && d.w == 1) {
            u32x2 fa, fb;
            asm volatile("global_load_dwordx2 %0, %2, %3 sc1\n\tglobal_load_dwordx2 %1, %2, %4 sc1\n\ts_waitcnt vmcnt(0)" : "=&v"(fa), "=&v"(fb)
                         : "v"(kvf_off(2 * l)), "s"(ws + WS_KVF), "s"(ws + WS_KVF + KVF_BRANCH) : "memory");
            if (!__all(fa[0] == tag && fa[1] == tag && fb[0] == tag && fb[1] == tag)) {
                poll2(ws + WS_KVF, kvf_off(2 * l), tag); poll2(ws + WS_KVF + KVF_BRANCH, kvf_off(2 * l), tag);
            }
        }
        WAIT_VM_BAR(0);
        if (pass || !__builtin_amdgcn_readfirstlane(*(volatile LAS unsigned*)(size_t)(lds + L_REDO))) break;
        dma_kv_first(d, lds, kv);
#pragma unroll 1
        for (int i = 0; i < 14; ++i) glds16_sc1(kv + kv_rest_off(d) + i * 1024, l * 16, lds + kv_rest_off(d) + i * 1024);
        init_const_rows(lds);
        WAIT_VM_BAR(0);
        f32x16 z;
#pragma unroll
        for (int i = 0; i < 16; ++i) z[i] = 0.f;
        float ta = -__builtin_inff(), tb = -__builtin_inff();
#pragma unroll 1
        for (int j = 0; j < 64; ++j) {
            const bf16x8 kf = as_bf(lds_read16(lds + L_K + j * 1024 + l * 16));
            ta = fmaxf(ta, max16(MFMA32(kf, as_bf(qa), z)));
            tb = fmaxf(tb, max16(MFMA32(kf, as_bf(qb), z)));
        }
        ma = half_swap_max(ta); mb = half_swap_max(tb);
    }
    if (blockIdx.x == 0 && threadIdx.x == 64) store4_sc1(ws + WS_EPOCH, 0, tag);

    const int ct = d.sub, rgl = d.w & 1, kq = d.w >> 1, br = kq >> 1;
    if (d.w == 1)
        asm volatile("s_mov_b64 exec, 0xff\n\tglobal_store_dword %0, %1, %2 sc1\n\ts_mov_b64 exec, -1"
                     :: "v"(l * 128 + d.sub * 4), "v"(tag), "s"(ws + WS_OF + d.rb * 1024) : "memory");
    if (d.w == 0) poll1(ws + WS_OF + d.rb * 1024 + d.sub * 128, (l & 7) * 4, tag);
    asm volatile("s_barrier" ::: "memory");
    const int h = l >> 5, g1 = (l >> 4) & 1, q = (l & 15) >> 2, p = l & 3;
    f32x16 acc0, acc1;
    float g;
    {
        const char* of = ws + WS_O + (size_t)((br * 64 + d.rb * 2 + rgl) * 32 + (kq & 1) * 16) * 1024;
        const unsigned kb = (4 * h + q) * 128 + 8 * (p & 1);
        const unsigned sw = ((q >> 1) & 1) << 2;
        const unsigned c0 = kb + (((2 * g1 + (p >> 1)) ^ sw) << 4), c1 = kb + (((4 + 2 * g1 + (p >> 1)) ^ sw) << 4);
        const unsigned s0 = wo_seg(lds, 4 * kq), s1 = wo_seg(lds, 4 * kq + 1), s2 = wo_seg(lds, 4 * kq + 2), s3 = wo_seg(lds, 4 * kq + 3);
        asm volatile(
        "global_load_dwordx4 v[128:131], %3, %5 sc1\n"
        "global_load_dwordx4 v[132:135], %3, %5 offset:1024 sc1\n"
        "global_load_dwordx4 v[136:139], %3, %5 offset:2048 sc1\n"
        "global_load_dwordx4 v[140:143], %3, %5 offset:3072 sc1\n"
        "global_load_dwordx4 v[144:147], %3, %6 sc1\n"
        "global_load_dwordx4 v[148:151], %3, %6 offset:1024 sc1\n"
        "global_load_dwordx4 v[152:155], %3, %6 offset:2048 sc1\n"
        "global_load_dwordx4 v[156:159], %3, %6 offset:3072 sc1\n"
        "global_load_dwordx4 v[160:163], %3, %7 sc1\n"
        "global_load_dwordx4 v[164:167], %3, %7 offset:1024 sc1\n"
        "global_load_dwordx4 v[168:171], %3, %7 offset:2048 sc1\n"
        "global_load_dwordx4 v[172:175], %3, %7 offset:3072 sc1\n"
        "global_load_dwordx4 v[176:179], %3, %8 sc1\n"
        "global_load_dwordx4 v[180:183], %3, %8 offset:1024 sc1\n"
        "global_load_dwordx4 v[184:187], %3, %8 offset:2048 sc1\n"
        "global_load_dwordx4 v[188:191], %3, %8 offset:3072 sc1\n"
        "global_load_dword %2, %4, %9 sc1\n"
        "ds_read_b64_tr_b16 v[192:193], %10 offset:0\n"
        "ds_read_b64_tr_b16 v[194:195], %10 offset:1024\n"
        "ds_read_b64_tr_b16 v[196:197], %11 offset:0\n"
        "ds_read_b64_tr_b16 v[198:199], %11 offset:1024\n"
        "ds_read_b64_tr_b16 v[200:201], %10 offset:2048\n"
        "ds_read_b64_tr_b16 v[202:203], %10 offset:3072\n"
        "ds_read_b64_tr_b16 v[204:205], %11 offset:2048\n"
        "ds_read_b64_tr_b16 v[206:207], %11 offset:3072\n"
        "ds_read_b64_tr_b16 v[208:209], %10 offset:4096\n"
        "ds_read_b64_tr_b16 v[210:211], %10 offset:5120\n"
        "ds_read_b64_tr_b16 v[212:213], %11 offset:4096\n"
        "ds_read_b64_tr_b16 v[214:215], %11 offset:5120\n"
        "s_waitcnt vmcnt(16) lgkmcnt(8)\n"
        "v_mfma_f32_32x32x16_bf16 %0, v[192:195], v[128:131], 0\n"
        "v_mfma_f32_32x32x16_bf16 %1, v[196:199], v[128:131], 0\n"
        "ds_read_b64_tr_b16 v[192:193], %10 offset:6144\n"
        "ds_read_b64_tr_b16 v[194:195], %10 offset:7168\n"
        "ds_read_b64_tr_b16 v[196:197], %11 offset:6144\n"
        "ds_read_b64_tr_b16 v[198:199], %11 offset:7168\n"
        "s_waitcnt vmcnt(15) lgkmcnt(8)\n"
        "v_mfma_f32_32x32x16_bf16 %0, v[200:203], v[132:135], %0\n"
        "v_mfma_f32_32x32x16_bf16 %1, v[204:207], v[132:135], %1\n"
        "ds_read_b64_tr_b16 v[200:201], %12 offset:0\n"
        "ds_read_b64_tr_b16 v[202:203], %12 offset:1024\n"
        "ds_read_b64_tr_b16 v[204:205], %13 offset:0\n"
        "ds_read_b64_tr_b16 v[206:207], %13 offset:1024\n"
        "s_waitcnt vmcnt(14) lgkmcnt(8)\n"
        "v_mfma_f32_32x32x16_bf16 %0, v[208:211], v[136:139], %0\n"
        "v_mfma_f32_32x32x16_bf16 %1, v[212:215], v[136:139], %1\n"
        "ds_read_b64_tr_b16 v[208:209], %12 offset:2048\n"
        "ds_read_b64_tr_b16 v[210:211], %12 offset:3072\n"
        "ds_read_b64_tr_b16 v[212:213], %13 offset:2048\n"
        "ds_read_b64_tr_b16 v[214:215], %13 offset:3072\n"
        "s_waitcnt vmcnt(13) lgkmcnt(8)\n"
        "v_mfma_f32_32x32x16_bf16 %0, v[192:195], v[140:143], %0\n"
        "v_mfma_f32_32x32x16_bf16 %1, v[196:199], v[140:143], %1\n"
        "ds_read_b64_tr_b16 v[192:193], %12 offset:4096\n"
        "ds_read_b64_tr_b16 v[194:195], %12 offset:5120\n"
        "ds_read_b64_tr_b16 v[196:197], %13 offset:4096\n"
        "ds_read_b64_tr_b16 v[198:199], %13 offset:5120\n"
        "s_waitcnt vmcnt(12) lgkmcnt(8)\n"
        "v_mfma_f32_32x32x16_bf16 %0, v[200:203], v[144:147], %0\n"
        "v_mfma_f32_32x32x16_bf16 %1, v[204:207], v[144:147], %1\n"
        "ds_read_b64_tr_b16 v[200:201], %12 offset:6144\n"
        "ds_read_b64_tr_b16 v[202:203], %12 offset:7168\n"
        "ds_read_b64_tr_b16 v[204:205], %13 offset:6144\n"
        "ds_read_b64_tr_b16 v[206:207], %13 offset:7168\n"
        "s_waitcnt vmcnt(11) lgkmcnt(8)\n"
        "v_mfma_f32_32x32x16_bf16 %0, v[208:211], v[148:151], %0\n"
        "v_mfma_f32_32x32x16_bf16 %1, v[212:215], v[148:151], %1\n"
        "ds_read_b64_tr_b16 v[208:209], %14 offset:0\n"
        "ds_read_b64_tr_b16 v[210:211], %14 offset:1024\n"
        "ds_read_b64_tr_b16 v[212:213], %15 offset:0\n"
        "ds_read_b64_tr_b16 v[214:215], %15 offset:1024\n"
        "s_waitcnt vmcnt(10) lgkmcnt(8)\n"
        "v_mfma_f32_32x32x16_bf16 %0, v[192:195], v[152:155], %0\n"
        "v_mfma_f32_32x32x16_bf16 %1, v[196:199], v[152:155], %1\n"
        "ds_read_b64_tr_b16 v[192:193], %14 offset:2048\n"
        "ds_read_b64_tr_b16 v[194:195], %14 offset:3072\n"
        "ds_read_b64_tr_b16 v[196:197], %15 offset:2048\n"
        "ds_read_b64_tr_b16 v[198:199], %15 offset:3072\n"
        "s_waitcnt vmcnt(9) lgkmcnt(8)\n"
        "v_mfma_f32_32x32x16_bf16 %0, v[200:203], v[156:159], %0\n"
        "v_mfma_f32_32x32x16_bf16 %1, v[204:207], v[156:159], %1\n"
        "ds_read_b64_tr_b16 v[200:201], %14 offset:4096\n"
        "ds_read_b64_tr_b16 v[202:203], %14 offset:5120\n"
        "ds_read_b64_tr_b16 v[204:205], %15 offset:4096\n"
        "ds_read_b64_tr_b16 v[206:207], %15 offset:5120\n"
        "s_waitcnt vmcnt(8) lgkmcnt(8)\n"
        "v_mfma_f32_32x32x16_bf16 %0, v[208:211], v[160:163], %0\n"
        "v_mfma_f32_32x32x16_bf16 %1, v[212:215], v[160:163], %1\n"
        "ds_read_b64_tr_b16 v[208:209], %14 offset:6144\n"
        "ds_read_b64_tr_b16 v[210:211], %14 offset:7168\n"
        "ds_read_b64_tr_b16 v[212:213], %15 offset:6144\n"
        "ds_read_b64_tr_b16 v[214:215], %15 offset:7168\n"
        "s_waitcnt vmcnt(7) lgkmcnt(8)\n"
        "v_mfma_f32_32x32x16_bf16 %0, v[192:195], v[164:167], %0\n"
        "v_mfma_f32_32x32x16_bf16 %1, v[196:199], v[164:167], %1\n"
        "ds_read_b64_tr_b16 v[192:193], %16 offset:0\n"
        "ds_read_b64_tr_b16 v[194:195], %16 offset:1024\n"
        "ds_read_b64_tr_b16 v[196:197], %17 offset:0\n"
        "ds_read_b64_tr_b16 v[198:199], %17 offset:1024\n"
        "s_waitcnt vmcnt(6) lgkmcnt(8)\n"
        "v_mfma_f32_32x32x16_bf16 %0, v[200:203], v[168:171], %0\n"
        "v_mfma_f32_32x32x16_bf16 %1, v[204:207], v[168:171], %1\n"
        "ds_read_b64_tr_b16 v[200:201], %16 offset:2048\n"
        "ds_read_b64_tr_b16 v[202:203], %16 offset:3072\n"
        "ds_read_b64_tr_b16 v[204:205], %17 offset:2048\n"
        "ds_read_b64_tr_b16 v[206:207], %17 offset:3072\n"
        "s_waitcnt vmcnt(5) lgkmcnt(8)\n"
        "v_mfma_f32_32x32x16_bf16 %0, v[208:211], v[172:175], %0\n"
        "v_mfma_f32_32x32x16_bf16 %1, v[212:215], v[172:175], %1\n"
        "ds_read_b64_tr_b16 v[208:209], %16 offset:4096\n"
        "ds_read_b64_tr_b16 v[210:211], %16 offset:5120\n"
        "ds_read_b64_tr_b16 v[212:213], %17 offset:4096\n"
        "ds_read_b64_tr_b16 v[214:215], %17 offset:5120\n"
        "s_waitcnt vmcnt(4) lgkmcnt(8)\n"
        "v_mfma_f32_32x32x16_bf16 %0, v[192:195], v[176:179], %0\n"
        "v_mfma_f32_32x32x16_bf16 %1, v[196:199], v[176:179], %1\n"
        "ds_read_b64_tr_b16 v[192:193], %16 offset:6144\n"
        "ds_read_b64_tr_b16 v[194:195], %16 offset:7168\n"
        "ds_read_b64_tr_b16 v[196:197], %17 offset:6144\n"
        "ds_read_b64_tr_b16 v[198:199], %17 offset:7168\n"
        "s_waitcnt vmcnt(3) lgkmcnt(8)\n"
        "v_mfma_f32_32x32x16_bf16 %0, v[200:203], v[180:183], %0\n"
        "v_mfma_f32_32x32x16_bf16 %1, v[204:207], v[180:183], %1\n"
        "s_waitcnt vmcnt(2) lgkmcnt(4)\n"
        "v_mfma_f32_32x32x16_bf16 %0, v[208:211], v[184:187], %0\n"
        "v_mfma_f32_32x32x16_bf16 %1, v[212:215], v[184:187], %1\n"
        "s_waitcnt vmcnt(1) lgkmcnt(0)\n"
        "v_mfma_f32_32x32x16_bf16 %0, v[192:195], v[188:191], %0\n"
        "v_mfma_f32_32x32x16_bf16 %1, v[196:199], v[188:191], %1\n"
        "s_waitcnt vmcnt(0)\n"
        "s_nop 15\n"
            : "=&v"(acc0), "=&v"(acc1), "=&v"(g)
            : "v"(l * 16), "v"((rgl * 32 + (l & 31)) * 4), "s"(of), "s"(of + 4096), "s"(of + 8192), "s"(of + 12288), "s"(ws + WS_G + d.b * 8192 + d.rb * 256),
              "v"(s0 + c0), "v"(s0 + c1), "v"(s1 + c0), "v"(s1 + c1), "v"(s2 + c0), "v"(s2 + c1), "v"(s3 + c0), "v"(s3 + c1)
            : "v128", "v129", "v130", "v131", "v132", "v133", "v134", "v135", "v136", "v137", "v138", "v139", "v140", "v141", "v142", "v143", "v144", "v145", "v146", "v147", "v148", "v149", "v150", "v151", "v152", "v153", "v154", "v155", "v156", "v157", "v158", "v159", "v160", "v161", "v162", "v163", "v164", "v165", "v166", "v167", "v168", "v169", "v170", "v171", "v172", "v173", "v174", "v175", "v176", "v177", "v178", "v179", "v180", "v181", "v182", "v183", "v184", "v185", "v186", "v187", "v188", "v189", "v190", "v191", "v192", "v193", "v194", "v195", "v196", "v197", "v198", "v199", "v200", "v201", "v202", "v203", "v204", "v205", "v206", "v207", "v208", "v209", "v210", "v211", "v212", "v213", "v214", "v215", "memory");
    }
    {
        const float f = br ? 1.f - g : g;
        const unsigned row = rgl * 32 + (l & 31);
        const unsigned pb = lds + part_base(kq) + row * 128;
#pragma unroll
        for (int gidx = 0; gidx < 4; ++gidx) {
            const u32x2 v0 = {pack2(acc0[4 * gidx] * f, acc0[4 * gidx + 1] * f), pack2(acc0[4 * gidx + 2] * f, acc0[4 * gidx + 3] * f)};
            const u32x2 v1 = {pack2(acc1[4 * gidx] * f, acc1[4 * gidx + 1] * f), pack2(acc1[4 * gidx + 2] * f, acc1[4 * gidx + 3] * f)};
            *(LAS u32x2*)(size_t)(pb + (((2 * gidx + h) ^ (row & 15)) << 3)) = v0;
            *(LAS u32x2*)(size_t)(pb + (((8 + 2 * gidx + h) ^ (row & 15)) << 3)) = v1;
        }
    }
    asm volatile("s_waitcnt lgkmcnt(0)\n\ts_barrier" ::: "memory");
    {
        const unsigned row = threadIdx.x >> 3, c8 = threadIdx.x & 7;
        const unsigned off = row * 128 + ((c8 ^ ((row & 15) >> 1)) << 4);
        float r[8];
#pragma unroll
        for (int i = 0; i < 8; ++i) r[i] = 0.f;
#pragma unroll
        for (int k = 0; k < 4; ++k) {
            const u32x4 p = *(const LAS u32x4*)(size_t)(lds + part_base(k) + off);
#pragma unroll
            for (int i = 0; i < 4; ++i) { r[2 * i] += __uint_as_float(p[i] << 16); r[2 * i + 1] += __uint_as_float(p[i] & 0xFFFF0000u); }
        }
        const bool sw = row & 1;
        const u32x4 o = {pack2(sw ? r[4] : r[0], sw ? r[5] : r[1]), pack2(sw ? r[6] : r[2], sw ? r[7] : r[3]),
                         pack2(sw ? r[0] : r[4], sw ? r[1] : r[5]), pack2(sw ? r[2] : r[6], sw ? r[3] : r[7])};
        asm volatile("global_store_dwordx4 %0, %1, %2 sc1\n\ts_nop 1" :: "v"(row * 1024 + c8 * 16), "v"(o), "s"(out + (size_t)d.rb * 65536 + ct * 128) : "memory");
    }
    asm volatile(".Lmqa_end:");
}

}

extern "C" void kernel_launch(void* const* d_in, const int* in_sizes, int n_in, void* d_out, int out_size, void* d_ws, size_t ws_size,
                              hipStream_t stream) {
    const char* p[11];
    for (int i = 0; i < 11; ++i) p[i] = (const char*)d_in[i];
    mqa_kernel<<<256, NTHR, 0, stream>>>(p[0], p[1], p[2], p[3], p[4], p[6], p[7], p[8], p[9], p[10], (char*)d_out, (char*)d_ws);
}
```
